```python
import torch

def _softmax(x, axis=-1):
    return torch.softmax(x, dim=axis)
def _gelu(x):
    return torch.nn.functional.gelu(x, approximate="tanh")
def _silu(x):
    return torch.nn.functional.silu(x)
def _sigmoid(x):
    return torch.sigmoid(x)
def _layernorm(x, axis=-1, eps=1e-5):
    m = x.mean(dim=axis, keepdim=True)
    v = x.var(dim=axis, keepdim=True, unbiased=False)
    return (x - m) / torch.sqrt(v + eps)
def _scatter_add(tgt, idx, src, axis=0):
    return tgt.clone().index_add_(axis, idx, src)
def _segment_sum(src, seg, n):
    out = torch.zeros((n,) + src.shape[1:], dtype=src.dtype)
    return out.index_add_(0, seg, src)
def _pad(x, pw):
    flat = tuple(v for pair in reversed(pw) for v in pair)
    return torch.nn.functional.pad(x, flat)

kernel_name = 'interleave_layers__linear_kernel_hedgehog__spectral_fnet__prompt'

def setup_inputs():

    _w = (lambda a: (lambda t: t.to(torch.bfloat16) if t.is_floating_point() else t)(torch.as_tensor(a)))
    def _A_setup():
        B, S, D, H = 1, 2048, 448, 16
        Dh = D // H
        return {
            "x": _w((lambda *s: torch.randn(*s, dtype=torch.bfloat16) * 0.4)(B, S, D)),
            "wqkv": _w((lambda *s: torch.randn(*s, dtype=torch.bfloat16) * 1.6)(D, 3 * D)),
            "w_phi": _w((lambda *s: torch.randn(*s, dtype=torch.bfloat16) * 1.6)(H, Dh, Dh) * (Dh ** -0.5)),
            "wo": _w((lambda *s: torch.randn(*s, dtype=torch.bfloat16) * 1.6)(D, D)),
        }
    _A_in = _A_setup()

    def _B_setup():
        B, S, D = 1, 2048, 448
        ns = torch.arange(S)[:, None] * torch.arange(S)[None, :]
        nd = torch.arange(D)[:, None] * torch.arange(D)[None, :]
        Ws_re = torch.cos(-2.0 * torch.pi * ns / S).to(float)
        Ws_im = torch.sin(-2.0 * torch.pi * ns / S).to(float)
        Wd_re = torch.cos(-2.0 * torch.pi * nd / D).to(float)
        Wd_im = torch.sin(-2.0 * torch.pi * nd / D).to(float)
        return {
            "x": _w((lambda *s: torch.randn(*s, dtype=torch.bfloat16) * 0.4)(B, S, D)),
            "dft_seq_re": _w(Ws_re),
            "dft_seq_im": _w(Ws_im),
            "dft_hid_re": _w(Wd_re),
            "dft_hid_im": _w(Wd_im),
        }
    _B_in = _B_setup()

    return {
        "A_x": _A_in["x"],
        "A_wqkv": _A_in["wqkv"],
        "A_w_phi": _A_in["w_phi"],
        "A_wo": _A_in["wo"],
        "B_x": _B_in["x"],
        "B_dft_seq_re": _B_in["dft_seq_re"],
        "B_dft_seq_im": _B_in["dft_seq_im"],
        "B_dft_hid_re": _B_in["dft_hid_re"],
        "B_dft_hid_im": _B_in["dft_hid_im"],
    }

def reference(A_x, A_wqkv, A_w_phi, A_wo, B_x, B_dft_seq_re, B_dft_seq_im, B_dft_hid_re, B_dft_hid_im):
    def _A_ref(x, wqkv, w_phi, wo):
        B, S, D = x.shape
        H = 16; Dh = D // H
        qkv = (lambda x, *d: x.permute(*d))((x @ wqkv).reshape(B, S, 3, H, Dh), 2, 0, 3, 1, 4)
        q, k, v = qkv[0], qkv[1], qkv[2]
        pq = torch.einsum("bhsd,hdf->bhsf", q, w_phi)
        pk = torch.einsum("bhsd,hdf->bhsf", k, w_phi)
        phi_q = torch.concatenate([_softmax(pq, axis=-1), _softmax(-pq, axis=-1)], axis=-1)
        phi_k = torch.concatenate([_softmax(pk, axis=-1), _softmax(-pk, axis=-1)], axis=-1)
        kv = torch.einsum("bhsf,bhsd->bhfd", phi_k, v)
        z  = torch.einsum("bhsf->bhf", phi_k)
        num = torch.einsum("bhqf,bhfd->bhqd", phi_q, kv)
        den = torch.einsum("bhqf,bhf->bhq", phi_q, z)[..., None] + 1e-6
        o = num / den
        return (lambda x, *d: x.permute(*d))(o, 0, 2, 1, 3).reshape(B, S, D) @ wo

    def _B_ref(x, dft_seq_re, dft_seq_im, dft_hid_re, dft_hid_im):
        a = torch.einsum("bsd,ed->bse", x, dft_hid_re)
        b = torch.einsum("bsd,ed->bse", x, dft_hid_im)
        y = torch.einsum("ts,bse->bte", dft_seq_re, a) - torch.einsum("ts,bse->bte", dft_seq_im, b)
        return y

    x = A_x
    x = x + _A_ref(_layernorm(x, axis=-1), A_wqkv, A_w_phi, A_wo)
    x = x + _B_ref(_layernorm(x, axis=-1), B_dft_seq_re, B_dft_seq_im, B_dft_hid_re, B_dft_hid_im)
    x = x + _A_ref(_layernorm(x, axis=-1), A_wqkv, A_w_phi, A_wo)
    x = x + _B_ref(_layernorm(x, axis=-1), B_dft_seq_re, B_dft_seq_im, B_dft_hid_re, B_dft_hid_im)
    return x
```

```cpp
#include <hip/hip_runtime.h>
#include <hip/hip_bf16.h>
#include <stdint.h>

namespace fused {

typedef unsigned v4u __attribute__((ext_vector_type(4)));
typedef unsigned v2u __attribute__((ext_vector_type(2)));
typedef float f32x2 __attribute__((ext_vector_type(2)));
typedef float f32x4 __attribute__((ext_vector_type(4)));
typedef float f32x16 __attribute__((ext_vector_type(16)));
typedef __bf16 bf16x2 __attribute__((ext_vector_type(2)));
typedef __bf16 bf16x8 __attribute__((ext_vector_type(8)));
typedef short s16x4 __attribute__((ext_vector_type(4)));
typedef __amdgpu_buffer_rsrc_t rsrc_t;
typedef unsigned long long u64;
#define LAS __attribute__((address_space(3)))
#define DI __device__ __forceinline__

constexpr unsigned MiB = 1u << 20;
constexpr unsigned WS_NONCE = 0;
constexpr unsigned WS_MB = 4096;
constexpr unsigned WS_G3 = 1 * MiB;
constexpr unsigned WS_KVP = 2 * MiB;
constexpr unsigned WS_OF = 4 * MiB;
constexpr unsigned WS_XN = 8 * MiB;
constexpr unsigned WS_PQ = 12 * MiB;
constexpr unsigned WS_X2 = 20 * MiB;
constexpr unsigned WS_SSQ = 22 * MiB;
constexpr unsigned WS_STAMP = 23 * MiB;
constexpr unsigned WS_BYTES = 24 * MiB;
constexpr unsigned OOB = 0x7FFFFFF0u;
constexpr int SC1 = 16;

constexpr unsigned L_WQ = 0, L_WO = 28672, L_MISC = 86016, L_SMALL = 102400, L_T = 106496, L_TOP = 155648;
constexpr unsigned L_WPHI = L_SMALL, L_CEX = L_SMALL + 1664, L_STAMP = L_SMALL + 3328;
constexpr unsigned L_HI = 50176;
constexpr unsigned LDS_BYTES = 163840;

struct Params {
  const void *x, *wqkv, *wphi, *wo, *cr, *ci, *hr, *hi;
  void *out, *ws;
};

#define MF16(c, a, b) asm volatile("v_mfma_f32_16x16x32_bf16 %0, %1, %2, %0" : "+v"(c) : "v"(a), "v"(b))
#define MF16z(c, a, b) asm volatile("v_mfma_f32_16x16x32_bf16 %0, %1, %2, 0" : "=&v"(c) : "v"(a), "v"(b))
#define nMF16(c, a, b) asm volatile("s_nop 1\n\tv_mfma_f32_16x16x32_bf16 %0, %1, %2, %0" : "+v"(c) : "v"(a), "v"(b))
#define MF32(c, a, b) asm volatile("v_mfma_f32_32x32x16_bf16 %0, %1, %2, %0" : "+v"(c) : "v"(a), "v"(b))
#define MF32z(c, a, b) asm volatile("v_mfma_f32_32x32x16_bf16 %0, %1, %2, 0" : "=&v"(c) : "v"(a), "v"(b))
#define nMF32(c, a, b) asm volatile("s_nop 1\n\tv_mfma_f32_32x32x16_bf16 %0, %1, %2, %0" : "+v"(c) : "v"(a), "v"(b))
#define nMF32z(c, a, b) asm volatile("s_nop 1\n\tv_mfma_f32_32x32x16_bf16 %0, %1, %2, 0" : "=&v"(c) : "v"(a), "v"(b))
#define MF16A(c, a, R) asm volatile("v_mfma_f32_16x16x32_bf16 %0, %1, a[%c2:%c3], %0" : "+v"(c) : "v"(a), "i"(4 * (R)), "i"(4 * (R) + 3))
#define MF16Az(c, a, R) asm volatile("v_mfma_f32_16x16x32_bf16 %0, %1, a[%c2:%c3], 0" : "=&v"(c) : "v"(a), "i"(4 * (R)), "i"(4 * (R) + 3))
#define FENCE1(c) asm volatile("s_nop 11" : "+v"(c))
#define FENCE2(c, d) asm volatile("s_nop 11" : "+v"(c), "+v"(d))
#define FENCE4(c) asm volatile("s_nop 7" : "+v"(c[0]), "+v"(c[1]), "+v"(c[2]), "+v"(c[3]))
#define PIN7(c) asm volatile("" : "+v"(c[0]), "+v"(c[1]), "+v"(c[2]), "+v"(c[3]), "+v"(c[4]), "+v"(c[5]), "+v"(c[6]))
#define A_LDS_RD(R, addr, off) asm volatile("ds_read_b128 a[%c0:%c1], %2 offset:%c3" ::"i"(4 * (R)), "i"(4 * (R) + 3), "v"(addr), "i"(off) : "memory")
#define A_LDS_WR(R, addr, off) asm volatile("ds_write_b128 %2, a[%c0:%c1] offset:%c3" ::"i"(4 * (R)), "i"(4 * (R) + 3), "v"(addr), "i"(off) : "memory")
#define A_GLD(R, rs, v, so) asm volatile("buffer_load_dwordx4 a[%c0:%c1], %2, %3, %4 offen nt" ::"i"(4 * (R)), "i"(4 * (R) + 3), "v"(v), "s"(rs), "s"(so) : "memory")
DI float asf(unsigned u) { return __builtin_bit_cast(float, u); }
DI unsigned asu(float f) { return __builtin_bit_cast(unsigned, f); }
DI unsigned pk2(float a, float b) { return __builtin_bit_cast(unsigned, __builtin_convertvector(f32x2{a, b}, bf16x2)); }
DI float lo16(unsigned u) { return asf(u << 16); }
DI float hi16(unsigned u) { return asf(u & 0xffff0000u); }
DI float dot2(unsigned a, unsigned b, float c) { return __builtin_amdgcn_fdot2_f32_bf16(__builtin_bit_cast(bf16x2, a), __builtin_bit_cast(bf16x2, b), c, false); }
DI v4u lds16(unsigned a) { return *(LAS const v4u*)(size_t)a; }
DI v2u lds8(unsigned a) { return *(LAS const v2u*)(size_t)a; }
DI float lds4(unsigned a) { return *(LAS const float*)(size_t)a; }
DI void sts16(unsigned a, v4u v) { *(LAS v4u*)(size_t)a = v; }
DI void sts8(unsigned a, v2u v) { *(LAS v2u*)(size_t)a = v; }
DI void sts4(unsigned a, float v) { *(LAS float*)(size_t)a = v; }
DI v2u tr8(unsigned a) { return __builtin_bit_cast(v2u, __builtin_amdgcn_ds_read_tr16_b64_v4i16((LAS s16x4*)(size_t)a)); }
DI v4u cat(v2u a, v2u b) { return v4u{a.x, a.y, b.x, b.y}; }
DI v4u tr16(unsigned a0, unsigned a1) { return cat(tr8(a0), tr8(a1)); }
template <int AUX> DI v4u gld16(rsrc_t r, unsigned v, unsigned s) { return __builtin_amdgcn_raw_buffer_load_b128(r, v, s, AUX); }
template <int AUX> DI v2u gld8(rsrc_t r, unsigned v, unsigned s) { return __builtin_amdgcn_raw_buffer_load_b64(r, v, s, AUX); }
template <int AUX> DI void gst16(v4u d, rsrc_t r, unsigned v, unsigned s) { __builtin_amdgcn_raw_buffer_store_b128(d, r, v, s, AUX); }
template <int AUX> DI void gst8(v2u d, rsrc_t r, unsigned v, unsigned s) { __builtin_amdgcn_raw_buffer_store_b64(d, r, v, s, AUX); }
DI rsrc_t mk(const void* p, unsigned bytes) { return __builtin_amdgcn_make_buffer_rsrc((void*)p, 0, bytes, 0x00020000); }
DI void dma16(rsrc_t r, unsigned v, unsigned s, unsigned lds) {
  asm volatile("s_mov_b32 m0, %3\n\ts_nop 0\n\tbuffer_load_dwordx4 %0, %1, %2 offen lds" ::"v"(v), "s"(r), "s"(s), "s"(lds) : "memory");
}
#define BAR() asm volatile("s_waitcnt lgkmcnt(0)\n\ts_barrier" ::: "memory")
#define DRAIN() asm volatile("s_waitcnt vmcnt(0)" ::: "memory")
#define LGKM0() asm volatile("s_waitcnt lgkmcnt(0)" ::: "memory")
#define SB() __builtin_amdgcn_sched_barrier(0)
#define UNROLL _Pragma("unroll")
DI unsigned opq(unsigned v) { asm volatile("" : "+v"(v)); return v; }
DI unsigned sopq(unsigned v) { asm volatile("" : "+s"(v)); return v; }
DI float sum32(float v) { auto r = __builtin_amdgcn_permlane32_swap(asu(v), asu(v), false, false); return asf(r[0]) + asf(r[1]); }
DI float sum16(float v) { auto r = __builtin_amdgcn_permlane16_swap(asu(v), asu(v), false, false); return asf(r[0]) + asf(r[1]); }
DI float max32(float v) { auto r = __builtin_amdgcn_permlane32_swap(asu(v), asu(v), false, false); return fmaxf(asf(r[0]), asf(r[1])); }
DI float min32(float v) { auto r = __builtin_amdgcn_permlane32_swap(asu(v), asu(v), false, false); return fminf(asf(r[0]), asf(r[1])); }

DI void post(rsrc_t ws, unsigned voff, u64 tag) {
  asm volatile("buffer_atomic_swap_x2 %0, %1, %2, 0 offen\n\ts_nop 1" ::"v"(tag), "v"(voff), "s"(ws) : "memory");
}
DI void poll(rsrc_t ws, unsigned voff, u64 tag) {
  unsigned cnt = 0; u64 t;
  asm volatile(
      "Lp%=:\n\tv_mov_b64 %1, 0\n\ts_nop 1\n\tbuffer_atomic_add_x2 %1, %2, %3, 0 offen sc0\n\ts_add_u32 %0, %0, 1\n\ts_waitcnt vmcnt(0)\n\t"
      "v_cmp_eq_u64 vcc, %4, %1\n\ts_nop 1\n\ts_cmp_eq_u64 vcc, exec\n\ts_cbranch_scc1 Ld%=\n\t"
      "s_cmp_lt_u32 %0, 0x10000\n\ts_cbranch_scc1 Lp%=\n\tLd%=:"
      : "+s"(cnt), "=&v"(t) : "v"(voff), "s"(ws), "s"(tag) : "vcc", "scc", "memory");
}

#define CSTAMP(i) do {} while (0)
#define STAMP(i) do {} while (0)

DI constexpr int rhoA(int i, int t) { return i < 4 ? (t ? 16 + 4 * i : 4 * i) : (i < 6 ? (t ? 48 + 4 * (i - 4) : 32 + 4 * (i - 4)) : (t ? 44 : 40)); }
DI constexpr int rhoO(int i, int t) { return i == 0 ? (t ? 8 : 0) : i == 1 ? (t ? 24 : 16) : i == 2 ? (t ? 12 : 4) : i == 3 ? (t ? 32 : 20) : i == 4 ? (t ? 36 : 28) : i == 5 ? (t ? 52 : 44) : (t ? 48 : 40); }

DI v4u pack8(const f32x16& x, int s) { return v4u{pk2(x[8 * s], x[8 * s + 1]), pk2(x[8 * s + 2], x[8 * s + 3]), pk2(x[8 * s + 4], x[8 * s + 5]), pk2(x[8 * s + 6], x[8 * s + 7])}; }

DI void dual_exp(const f32x16& p, bool h, float sc, float (&ep)[16], float (&en)[16], float& sp, float& sn) {
  float q[16];
  UNROLL for (int i = 0; i < 16; ++i) q[i] = (i >= 12 && h) ? p[0] : p[i];
  float mx = q[0], mn = q[0];
  UNROLL for (int i = 1; i < 16; ++i) { mx = fmaxf(mx, q[i]); mn = fminf(mn, q[i]); }
  mx = max32(mx); mn = min32(mn);
  const float a = -mx * sc, b = mn * sc;
  sp = 0.f; sn = 0.f;
  UNROLL for (int i = 0; i < 16; ++i) {
    float e = __builtin_amdgcn_exp2f(__builtin_fmaf(q[i], sc, a)), f = __builtin_amdgcn_exp2f(__builtin_fmaf(q[i], -sc, b));
    if (i >= 12) { e = h ? 0.f : e; f = h ? 0.f : f; }
    ep[i] = e; en[i] = f; sp += e; sn += f;
  }
  sp = sum32(sp); sn = sum32(sn);
}

#define S3SLEEP 4
#define B1AUX SC1
#define ROTT 2
struct Role {
  unsigned w, a_p, a_h, par, b_q, b_j, d_t, d_j, rot;
  DI Role(unsigned c0, unsigned w0) {
    const unsigned c = sopq(c0), xcd = c & 7, rr = c >> 3;
    w = sopq(w0);
    a_p = 4 * (xcd >> 1) + (rr >> 3); a_h = 8 * (xcd & 1) + (rr & 7); par = a_h & 1;
    b_q = 4 * xcd + (rr >> 3); b_j = rr & 7;
    d_t = 8 * xcd + (rr >> 2); d_j = rr & 3;
    rot = (4 * d_j + ROTT * (d_t & 7)) & 15;
  }
};

DI unsigned w_voff(unsigned l) { return (l >> 2) * 2688 + (l & 3) * 16; }
DI void stage_w1(const Params& P, const Role& R, unsigned v, unsigned part, unsigned n, unsigned dst) {
  dma16(mk(P.wqkv, 448 * 1344 * 2), v, n * (16 * 2688) + part * 896 + ((56 * R.a_h) & ~15u), dst + n * 1024);
}
#define FOR_3WAVES(N, n) _Pragma("unroll 1") for (unsigned n = R.w ? R.w - 1 : (N); n < (N); n += 3)
DI void patch_pad(const Role& R, unsigned l) {
  const unsigned a = L_T + (112 * R.w + l) * 64 + (R.par ? 0 : 56);
  sts8(a, v2u{0x00003f80u, 0u}); sts8(a + 28672, v2u{0u, 0u});
  if (l < 48) { sts8(a + 4096, v2u{0x00003f80u, 0u}); sts8(a + 4096 + 28672, v2u{0u, 0u}); }
}
DI unsigned wo_voff(unsigned l, int hf) { return (l >> 3) * 896 + 16 * (2 * (((l & 7) >> 1) ^ (((l >> 4) & 1) | (hf << 1))) + (l & 1)); }
DI void stage_h(const Params& P, const Role& R, unsigned l) {
  const unsigned v0 = (l >> 3) * 896 + 16 * ((l & 7) ^ (l >> 4)), v1 = (l >> 3) * 896 + 16 * ((l & 7) ^ (4 + (l >> 4)));
  FOR_3WAVES(14, n) {
    const unsigned m = n >= 7, u = n - 7 * m;
    const rsrc_t rH = mk(m ? P.hi : P.hr, 448 * 448 * 2);
    UNROLL for (int o = 0; o < 7; ++o) dma16(rH, (o & 1) ? v1 : v0, (56 * R.b_j + 8 * o) * 896 + u * 128, n * 7168 + o * 1024);
  }
}
DI unsigned a_soff(const Role& R, unsigned w, unsigned n) { return (32 * R.d_t + 16 * ((n ^ w) & 1)) * 4096 + 1024 * w + 128 * (((n >> 2) + (R.rot >> 1)) & 7); }
DI unsigned a_voff(unsigned l, int o) { return (8 * o + (l >> 3)) * 4096 + 16 * ((l & 7) ^ ((4 * o + (l >> 4)) & 7)); }
DI unsigned a_slot(unsigned w, unsigned j) { return j < 6 ? L_T + w * 12288 + j * 2048 : L_WQ + w * 6144 + (j - 6) * 2048; }
DI void a_piece(const Params& P, const Role& R, unsigned v0, unsigned v1, unsigned w, unsigned n, unsigned j) {
  const rsrc_t r = mk(((n ^ w) & 2) ? P.ci : P.cr, 2048 * 2048 * 2);
  const unsigned so = a_soff(R, w, n), dst = a_slot(w, j);
  dma16(r, v0, so, dst); dma16(r, v1, so, dst + 1024);
}
DI void a_issue9(const Params& P, const Role& R, unsigned l, unsigned n0) {
  const unsigned v0 = a_voff(l, 0), v1 = a_voff(l, 1);
  FOR_3WAVES(36, i) a_piece(P, R, v0, v1, i & 3, n0 + (i >> 2), i >> 2);
}
template <int N0, int N1> DI void a_read(const Role& R, unsigned l) {
  const unsigned t = l & 15, f = (t >> 1) & 7, b0 = t * 128 + 16 * ((l >> 4) ^ f), b1 = t * 128 + 16 * ((4 + (l >> 4)) ^ f);
  const unsigned x0 = b0 + L_T + R.w * 12288, x1 = b1 + L_T + R.w * 12288, y0 = b0 + L_WQ + R.w * 6144, y1 = b1 + L_WQ + R.w * 6144;
  UNROLL for (int n = N0; n < N1; ++n) {
    const int j = n - N0, r = 8 * (n >> 2) + (n & 3);
    if (j < 6) { A_LDS_RD(r, x0, j * 2048); A_LDS_RD(r + 4, x1, j * 2048); }
    else { A_LDS_RD(r, y0, (j - 6) * 2048); A_LDS_RD(r + 4, y1, (j - 6) * 2048); }
  }
  LGKM0();
}
DI float colsum_part(unsigned base, unsigned w, unsigned l) {
  const unsigned a = opq(base + (112 * w + 56 * (l >> 5) + ((l & 15) >> 2)) * 64 + ((l >> 4) & 1) * 32 + (l & 3) * 8);
  float c0 = 0.f, c1 = 0.f;
  UNROLL for (int i = 0; i < 14; ++i) { const v2u t = tr8(a + i * 256); c0 = dot2(t.x, 0x3f803f80u, c0); c1 = dot2(t.y, 0x3f803f80u, c1); }
  return sum32(c0 + c1);
}
DI unsigned colsum_all(unsigned l, int part) {
  const unsigned cx = opq(L_CEX + (l & 31) * 4);
  float s = 0.f;
  UNROLL for (int i = 0; i < 4; ++i) s += lds4(cx + (i * 3 + part) * 128);
  return pk2(s, s - lo16(pk2(s, s)));
}

__global__ void __launch_bounds__(256, 1) fused_fwd(Params P) {
  __shared__ __attribute__((aligned(1024))) char smem[LDS_BYTES];
  asm volatile("" ::"v"((unsigned)(size_t)(LAS char*)smem) : "memory", "a255");
  const unsigned lane = threadIdx.x & 63;
  const unsigned w0 = __builtin_amdgcn_readfirstlane(threadIdx.x >> 6), c0 = blockIdx.x;
  const rsrc_t rWS = mk(P.ws, WS_BYTES);
  STAMP(0); CSTAMP(29);

  v2u res[4];
  v4u xf[4][7];
  f32x16 kT, vv;
  float ssq;
  unsigned csk, csv, csq = 0;
  const u64 nonce = *(const __attribute__((address_space(4))) u64*)P.ws;
  auto kv_gemm = [&](unsigned l, unsigned w, auto&& hook) {
    const unsigned wl = L_T + (56 * (l >> 5) + ((l & 15) >> 2)) * 64 + ((l >> 4) & 1) * 32 + (l & 3) * 8;
    v4u wk[3], wv[3];
    unsigned a;
    auto rd = [&](int s) {
      if (s % 7 == 0) a = opq(wl + ((s / 7 + w) & 3) * 7168);
      wk[s % 3] = tr16(a + rhoA(s % 7, 0) * 64, a + rhoA(s % 7, 1) * 64); wv[s % 3] = tr16(a + 28672 + rhoA(s % 7, 0) * 64, a + 28672 + rhoA(s % 7, 1) * 64);
    };
    rd(0); rd(1);
    SB();
    UNROLL for (int s = 0; s < 28; ++s) {
      const v4u x = xf[s / 7][s % 7], k = wk[s % 3], b = wv[s % 3];
      if (s == 0) { MF32z(kT, k, x); } else { MF32(kT, k, x); }
      if (s + 2 < 28) rd(s + 2);
      SB();
      if (s == 0) { MF32z(vv, x, b); } else { MF32(vv, x, b); }
      hook(s);
      SB();
    }
    FENCE2(kT, vv);
  };

  {
    const Role R(c0, w0);
    const unsigned l = opq(lane) & 63, w = R.w;
    const rsrc_t rX = mk(P.x, 2048 * 448 * 2);
    const unsigned wv0 = w_voff(l);
    UNROLL for (int n = 0; n < 7; ++n) stage_w1(P, R, wv0, 1, 7 * w + n, L_T);
    UNROLL for (int n = 0; n < 7; ++n) stage_w1(P, R, wv0, 2, 7 * w + n, L_T + 28672);
    const unsigned win = L_WO + w * 14336, xs = (128 * R.a_p + 32 * w) * 896;
    unsigned xv[7];
    v4u xp[2][7];
    UNROLL for (int n = 0; n < 7; ++n) { const unsigned q = 64 * n + l, row = (q * 4682) >> 16, ch = q - 14 * row; xv[n] = row * 896 + 16 * (ch ^ ((row >> 3) & 1)); }
    UNROLL for (int Js = 0; Js < 2; ++Js)
      UNROLL for (int n = 0; n < 7; ++n) dma16(rX, xv[n], xs + 224 * ((Js + w) & 3), win + Js * 7168 + n * 1024);
    UNROLL for (int Js = 0; Js < 2; ++Js)
      UNROLL for (int n = 0; n < 7; ++n)
        asm volatile("buffer_load_dwordx4 %0, %1, %2, %3 offen" : "=&v"(xp[Js][n]) : "v"(xv[n]), "s"(rX), "s"(xs + 224 * ((Js + 2 + w) & 3)) : "memory");
    const unsigned row = l & 31, xb = opq(win + row * 224), sw = ((row >> 3) & 1) << 4, hb = 112 * (l >> 5);
    unsigned xo[7][2];
    UNROLL for (int i = 0; i < 7; ++i) { xo[i][0] = xb + ((hb + 2 * rhoA(i, 0)) ^ sw); xo[i][1] = xb + ((hb + 2 * rhoA(i, 1)) ^ sw); }
    STAMP(31);
    asm volatile("s_waitcnt vmcnt(28)" ::: "memory");
    patch_pad(R, l);
    {
      const float ck = colsum_part(L_T, w, l), cv = colsum_part(L_T + 28672, w, l);
      sts4(L_CEX + (w * 3 + (l >> 5)) * 128 + (l & 31) * 4, (l >> 5) ? cv : ck);
    }
    asm volatile("s_waitcnt vmcnt(14)" ::: "memory");
    STAMP(32);
    UNROLL for (int Js = 0; Js < 2; ++Js) {
      UNROLL for (int i = 0; i < 7; ++i) xf[Js][i] = cat(lds8(xo[i][0] + Js * 7168), lds8(xo[i][1] + Js * 7168));
      asm volatile("" ::: "memory");
    }
    asm volatile("s_waitcnt vmcnt(0)" : "+v"(xp[0][0]), "+v"(xp[0][1]), "+v"(xp[0][2]), "+v"(xp[0][3]), "+v"(xp[0][4]), "+v"(xp[0][5]), "+v"(xp[0][6]),
                 "+v"(xp[1][0]), "+v"(xp[1][1]), "+v"(xp[1][2]), "+v"(xp[1][3]), "+v"(xp[1][4]), "+v"(xp[1][5]), "+v"(xp[1][6])::"memory");
    const unsigned lin = opq(win + l * 16);
    UNROLL for (int Js = 0; Js < 2; ++Js)
      UNROLL for (int n = 0; n < 7; ++n) sts16(lin + Js * 7168 + n * 1024, xp[Js][n]);
    float q0 = 0.f, q1 = 0.f;
    auto sq = [&](int Js) { UNROLL for (int i = 0; i < 7; ++i) { const v4u x = xf[Js][i]; q0 = dot2(x.x, x.x, q0); q1 = dot2(x.y, x.y, q1); q0 = dot2(x.z, x.z, q0); q1 = dot2(x.w, x.w, q1); } };
    UNROLL for (int Js = 0; Js < 2; ++Js) {
      UNROLL for (int i = 0; i < 7; ++i) xf[2 + Js][i] = cat(lds8(xo[i][0] + Js * 7168), lds8(xo[i][1] + Js * 7168));
      asm volatile("" ::: "memory");
    }
    sq(0); sq(1); sq(2); sq(3);
    ssq = q0 + q1;
    BAR();
    STAMP(1);
    csk = colsum_all(l, 0); csv = colsum_all(l, 1);
    const rsrc_t rP = mk(P.wphi, 16 * 784 * 2);
    const rsrc_t r0 = mk((w >> 1) ? P.ci : P.cr, 2048 * 2048 * 2), r1 = mk((w >> 1) ? P.cr : P.ci, 2048 * 2048 * 2);
    const unsigned av0 = a_voff(l, 0), av1 = a_voff(l, 1);
    const unsigned ro = (64 * R.b_q + 16 * w + (l & 15)) * 896 + (56 * R.b_j + 4 * (l >> 4)) * 2;
    kv_gemm(l, w, [&](int s) {
      if (s < 7) stage_w1(P, R, wv0, 0, 7 * w + s, L_WQ);
      else if (s == 7) { if ((w & 1) * 64 + l < 98) dma16(rP, ((w & 1) * 64 + l) * 16, R.a_h * 1568, L_WPHI + (w & 1) * 1024); }
      else if (s < 12) asm volatile("buffer_load_dwordx2 %0, %1, %2, 0 offen" : "=&v"(res[s - 8]) : "v"((s == 11 && l >= 32) ? OOB : ro + 32 * (s - 8)), "s"(rX) : "memory");
      else {
        const int n = (s - 12) >> 1;
        if (s & 1) A_GLD(8 * (n >> 2) + (n & 3) + 4, (n & 2) ? r1 : r0, av1, a_soff(R, w, n));
        else A_GLD(8 * (n >> 2) + (n & 3), (n & 2) ? r1 : r0, av0, a_soff(R, w, n));
      }
    });
    asm volatile("s_waitcnt vmcnt(16)" : "+v"(res[0]), "+v"(res[1]), "+v"(res[2]), "+v"(res[3])::"memory");
  }

  _Pragma("unroll 1") for (unsigned pair = 0; pair < 2; ++pair) {
    const u64 tag0 = nonce + 1 + 6 * pair;
    v4u phq[4]; float spq;
    {
      const Role R(c0, w0);
      const unsigned l = opq(lane) & 63, h = l >> 5, w = R.w, par = R.par;
      STAMP(2 + 14 * pair);
      float mean;
      {
        const float sx = par ? kT[0] : kT[12];
        auto r = __builtin_amdgcn_permlane32_swap(asu(sx), asu(sx), false, false);
        mean = asf(par ? r[0] : r[1]) * (1.f / 448.f);
      }
      const float var = fmaxf(sum32(ssq) * (1.f / 448.f) - mean * mean, 0.f);
      const float sig = __builtin_amdgcn_sqrtf(var + 1e-5f), rstd = __builtin_amdgcn_rcpf(sig);
      v4u xa;
      {
        const unsigned mh = pk2(-mean, -mean), ml = pk2(-mean - lo16(mh), -mean - lo16(mh));
        const unsigned sh = pk2(sig, sig), sl = pk2(sig, sig - lo16(sh));
        xa = v4u{h ? 0u : mh, h ? 0u : ml, h ? 0u : sl, 0u};
      }
      {
        const unsigned pd = par ? 0 : 28;
        const unsigned one = (l & 31) == pd ? 0x00003f80u : ((l & 31) == pd + 1 ? 0x3f800000u : 0u);
        const v4u a1 = v4u{csk, csk, 0u, 0u}, a2 = v4u{csv, csv, one, 0u};
        nMF32(kT, a1, xa); nMF32(vv, xa, a2);
      }
      BAR();
      v4u wp[2];
      {
        const int q4 = (l & 15) >> 2, fq = 4 * ((l >> 4) & 1) + (l & 3);
        UNROLL for (int s = 0; s < 2; ++s) {
          v2u t2[2];
          UNROLL for (int t = 0; t < 2; ++t) {
            const int d0 = 16 * s + 8 * t + 4 * (int)h - 4 * (int)par;
            const bool ok = d0 >= 0 && d0 < 28;
            const v2u r = tr8(L_WPHI + ((ok ? d0 : 0) + q4) * 56 + (fq < 7 ? fq : 0) * 8);
            const bool keep = ok && (l & 31) < 28;
            t2[t] = v2u{keep ? r.x : 0u, keep ? r.y : 0u};
          }
          wp[s] = cat(t2[0], t2[1]);
        }
      }
      const float sc = rstd * 1.44269504088896f;
      {
        FENCE2(kT, vv);
        f32x16 pk;
        const v4u k0 = pack8(kT, 0), k1 = pack8(kT, 1);
        nMF32z(pk, wp[0], k0); nMF32(pk, wp[1], k1);
        sts16(L_TOP + w * 2048 + l * 16, pack8(vv, 0)); sts16(L_TOP + w * 2048 + 1024 + l * 16, pack8(vv, 1));
        FENCE1(pk);
        float ep[16], en[16], sp, sn;
        dual_exp(pk, h, sc, ep, en, sp, sn);
        const float fp = rstd * __builtin_amdgcn_rcpf(sp), fn = rstd * __builtin_amdgcn_rcpf(sn);
        const unsigned tok = l & 31, sz = (tok & 7) | ((((tok >> 1) ^ (tok >> 3)) & 1) << 3), pb = L_MISC + w * 4096 + tok * 128;
        UNROLL for (int G = 0; G < 4; ++G) {
          sts8(pb + 8 * ((2 * G + h) ^ sz), v2u{pk2(ep[4 * G] * fp, ep[4 * G + 1] * fp), pk2(ep[4 * G + 2] * fp, ep[4 * G + 3] * fp)});
          sts8(pb + 8 * ((8 + 2 * G + h) ^ sz), v2u{pk2(en[4 * G] * fn, en[4 * G + 1] * fn), pk2(en[4 * G + 2] * fn, en[4 * G + 3] * fn)});
        }
      }
      BAR();
      {
        const unsigned g = l >> 4, q4 = (l & 15) >> 2, U = (w >> 1) * 8 + 4 * (w & 1) + (l & 3);
        const unsigned t0 = 16 * (g >> 1) + 4 * (g & 1) + q4, lo3 = 4 * (g & 1) + q4;
        const unsigned p0 = opq(L_MISC + t0 * 128 + 8 * (U ^ (lo3 | (((q4 >> 1) & 1) << 3)))), p1 = opq(L_MISC + (t0 + 8) * 128 + 8 * (U ^ (lo3 | ((((q4 >> 1) & 1) ^ 1) << 3))));
        const unsigned vb = opq(L_TOP + (g >> 1) * 1024 + ((g & 1) * 32 + (l & 15)) * 16);
        v4u a[4], b0[4], b1[4];
        UNROLL for (int i = 0; i < 4; ++i) { a[i] = tr16(p0 + i * 4096, p1 + i * 4096); b0[i] = lds16(vb + i * 2048); b1[i] = lds16(vb + i * 2048 + 256); }
        f32x4 d0, d1;
        MF16z(d0, a[0], b0[0]); MF16z(d1, a[0], b1[0]);
        UNROLL for (int i = 1; i < 4; ++i) { MF16(d0, a[i], b0[i]); MF16(d1, a[i], b1[i]); }
        asm volatile("s_nop 7" : "+v"(d0), "+v"(d1));
        auto r0 = __builtin_amdgcn_permlane32_swap(pk2(d0[0], d0[1]), pk2(d1[0], d1[1]), false, false);
        auto r1 = __builtin_amdgcn_permlane32_swap(pk2(d0[2], d0[3]), pk2(d1[2], d1[3]), false, false);
        gst16<SC1>(v4u{r0[0], r1[0], r0[1], r1[1]}, rWS, ((g & 1) * 32 + (l >> 5) * 16 + (l & 15)) * 16, WS_KVP + pair * MiB + (R.a_h * 16 + R.a_p) * 4096 + w * 1024);
      }
      STAMP(3 + 14 * pair);
      {
        f32x16 qT;
        v4u wq[5];
        unsigned a;
        const unsigned wl = L_WQ + (56 * h + ((l & 15) >> 2)) * 64 + ((l >> 4) & 1) * 32 + (l & 3) * 8;
        auto rd = [&](int s) { if (s % 7 == 0) a = opq(wl + ((s / 7 + w) & 3) * 7168); wq[s % 5] = tr16(a + rhoA(s % 7, 0) * 64, a + rhoA(s % 7, 1) * 64); };
        const rsrc_t rWO = mk(P.wo, 448 * 448 * 2);
        const unsigned ov0 = wo_voff(l, 0), ov1 = wo_voff(l, 1);
        rd(0); rd(1); rd(2); rd(3);
        SB();
        UNROLL for (int s = 0; s < 28; ++s) {
          if (s == 0) { MF32z(qT, wq[0], xf[0][0]); } else { MF32(qT, wq[s % 5], xf[s / 7][s % 7]); }
          if (s + 4 < 28) rd(s + 4);
          if (s & 1) dma16(rWO, s < 14 ? ov0 : ov1, (14 * w + (s >> 1)) * 8 * 896 + 112 * R.b_j, L_WO + (14 * w + (s >> 1)) * 1024);
          SB();
        }
        if (pair == 0) { const float cq = colsum_part(L_WQ, w, l); if (h == 0) sts4(L_CEX + (w * 3 + 2) * 128 + l * 4, cq); }
        asm volatile("s_waitcnt vmcnt(14)" ::: "memory");
        BAR();
        if (w == 1 && l < 16) post(rWS, WS_MB + (l * 16 + R.a_h) * 512 + R.a_p * 8, tag0);
        if (pair == 0) csq = colsum_all(l, 2);
        const v4u a0 = v4u{csq, csq, 0u, 0u};
        nMF32(qT, a0, xa);
        FENCE1(qT);
        f32x16 pq;
        const v4u q0 = pack8(qT, 0), q1 = pack8(qT, 1);
        nMF32z(pq, wp[0], q0); nMF32(pq, wp[1], q1);
        FENCE1(pq);
        float ep[16], en[16], sn;
        dual_exp(pq, h, sc, ep, en, spq, sn);
        const float fn = spq * __builtin_amdgcn_rcpf(sn);
        UNROLL for (int s = 0; s < 2; ++s) {
          phq[s] = v4u{pk2(ep[8 * s], ep[8 * s + 1]), pk2(ep[8 * s + 2], ep[8 * s + 3]), pk2(ep[8 * s + 4], ep[8 * s + 5]), pk2(ep[8 * s + 6], ep[8 * s + 7])};
          phq[2 + s] = v4u{pk2(en[8 * s] * fn, en[8 * s + 1] * fn), pk2(en[8 * s + 2] * fn, en[8 * s + 3] * fn), pk2(en[8 * s + 4] * fn, en[8 * s + 5] * fn), pk2(en[8 * s + 6] * fn, en[8 * s + 7] * fn)};
        }
        asm volatile("" : "+v"(phq[0]), "+v"(phq[1]), "+v"(phq[2]), "+v"(phq[3]), "+v"(spq));
      }
      if (pair == 0) a_issue9(P, R, l, 8);
      STAMP(4 + 14 * pair);
      if (w == 0 && l < 16) poll(rWS, WS_MB + (R.a_p * 16 + R.a_h) * 512 + l * 8, tag0);
      BAR();
    }
    STAMP(5 + 14 * pair);
    {
      const Role R(c0, w0);
      const unsigned l = opq(lane) & 63, h = l >> 5, w = R.w, par = R.par;
      v4u pt[16];
      UNROLL for (int i = 0; i < 16; ++i) { pt[i] = gld16<SC1>(rWS, l * 16, WS_KVP + pair * MiB + (R.a_h * 16 + i) * 4096 + w * 1024); SB(); }
      {
        v4u sel;
        UNROLL for (int e = 0; e < 4; ++e) {
          const unsigned r0 = ((2 * e) & 3) + 8 * ((2 * e) >> 2) + 4 * h;
          sel[e] = (l & 31) == r0 ? 0x00003f80u : ((l & 31) == r0 + 1 ? 0x3f800000u : 0u);
        }
        f32x16 t;
        nMF32z(t, sel, pt[0]);
        UNROLL for (int i = 1; i < 16; ++i) MF32(t, sel, pt[i]);
        FENCE1(t);
        sts16(L_MISC + w * 1024 + l * 16, pack8(t, 0));
      }
      BAR();
      f32x16 n;
      v4u kf[4];
      const unsigned kb = opq(L_MISC + l * 16);
      UNROLL for (int i = 0; i < 4; ++i) kf[i] = lds16(kb + i * 1024);
      MF32z(n, kf[0], phq[0]); MF32(n, kf[1], phq[1]); MF32(n, kf[2], phq[2]); MF32(n, kf[3], phq[3]);
      FENCE1(n);
      const float ds = par ? n[0] + n[1] : n[12] + n[13];
      auto r = __builtin_amdgcn_permlane32_swap(asu(ds), asu(ds), false, false);
      const float den = asf(par ? r[0] : r[1]);
      const float inv = __builtin_amdgcn_rcpf(den + 1e-6f * spq);
      v2u o[4];
      UNROLL for (int G = 0; G < 4; ++G) o[G] = v2u{pk2(n[4 * G] * inv, n[4 * G + 1] * inv), pk2(n[4 * G + 2] * inv, n[4 * G + 3] * inv)};
      const unsigned Pp = R.a_h >> 1;
      const unsigned so = WS_OF + pair * 2 * MiB + (8 * R.a_p + 2 * w) * 14336 + 7 * (Pp >> 2) * 1024 + (Pp & 3) * 256;
      const unsigned vo = ((l >> 4) & 1) * 14336 + (l & 15) * 16;
      const bool full2 = h == par, useB = par && !h;
      gst16<SC1>(useB ? cat(o[2], o[3]) : cat(o[0], o[1]), rWS, vo + (par ? (h ? 4 : 6) : (h ? 2 : 0)) * 1024, so);
      gst16<SC1>(cat(o[2], o[3]), rWS, full2 ? vo + (par ? 5 : 1) * 1024 : OOB, so);
      gst8<SC1>(par ? o[1] : o[2], rWS, full2 ? OOB : vo + 3 * 1024 + 8 * par, so);
      STAMP(6 + 14 * pair);
      if (pair == 0) a_read<8, 17>(R, l);
      DRAIN();
      BAR();
      if (w == 1 && l < 16) post(rWS, WS_MB + 131072 + ((2 * R.a_p + (l >> 3)) * 8 + (l & 7)) * 512 + R.a_h * 8, tag0 + 1);
      if (pair == 0) a_issue9(P, R, l, 17);
    }
    {
      const Role R(c0, w0);
      const unsigned l = opq(lane) & 63, g = l >> 4, q4 = (l & 15) >> 2, w = R.w;
      f32x4 acc[4];
      {
        constexpr int PRE = 8;
        const unsigned f = ((q4 >> 1) & 1) | ((g & 1) << 1), wb = L_WO + (56 * g + q4) * 128 + (l & 3) * 8;
        unsigned ba[4];
        UNROLL for (int a = 0; a < 4; ++a) ba[a] = opq(wb + 32 * (a ^ f));
        v4u wf[PRE][4];
        auto rd = [&](int s, int a) { wf[s % PRE][a] = tr16(ba[a] + (224 * (s / 7) + rhoO(s % 7, 0)) * 128, ba[a] + (224 * (s / 7) + rhoO(s % 7, 1)) * 128); };
        UNROLL for (int s = 0; s < PRE; ++s) UNROLL for (int a = 0; a < 4; ++a) rd(s, a);
        UNROLL for (int a = 0; a < 4; ++a) acc[a] = f32x4{lo16(res[a].x), hi16(res[a].x), lo16(res[a].y), hi16(res[a].y)};
        asm volatile("" : "+v"(acc[0]), "+v"(acc[1]), "+v"(acc[2]), "+v"(acc[3]));
        if (w == 0 && l < 16) poll(rWS, WS_MB + 131072 + (R.b_q * 8 + R.b_j) * 512 + l * 8, tag0 + 1);
        BAR();
        STAMP(7 + 14 * pair);
        v4u of[14];
        UNROLL for (int s = 0; s < 14; ++s) { of[s] = gld16<SC1>(rWS, l * 16 + (s & 3) * 1024, WS_OF + pair * 2 * MiB + (4 * R.b_q + w) * 14336 + (s >> 2) * 4096); SB(); }
        UNROLL for (int s = 0; s < 14; ++s)
          UNROLL for (int a = 0; a < 4; ++a) { MF16(acc[a], wf[s % PRE][a], of[s]); if (s + PRE < 14) rd(s + PRE, a); SB(); }
        FENCE4(acc);
      }
      UNROLL for (int e = 0; e < 4; ++e) acc[3][e] = l >= 32 ? 0.f : acc[3][e];
      float s1 = 0.f, s2 = 0.f;
      UNROLL for (int a = 0; a < 4; ++a) UNROLL for (int e = 0; e < 4; ++e) { s1 += acc[a][e]; s2 = __builtin_fmaf(acc[a][e], acc[a][e], s2); }
      s1 = sum32(sum16(s1)); s2 = sum32(sum16(s2));
      const unsigned tg = (unsigned)tag0 + 2;
      const unsigned g3 = WS_G3 + pair * 262144 + R.b_q * 8192 + w * 256;
      gst16<SC1>(v4u{asu(s1), tg, asu(s2), tg}, rWS, l < 16 ? l * 16 : OOB, g3 + R.b_j * 1024);
      UNROLL for (int a = 0; a < 4; ++a) res[a] = v2u{pk2(acc[a][0], acc[a][1]), pk2(acc[a][2], acc[a][3])};
      STAMP(8 + 14 * pair);
      if (pair == 0) {
        BAR();
        a_read<17, 26>(R, l);
        const unsigned v0 = a_voff(l, 0), v1 = a_voff(l, 1);
        _Pragma("unroll 1") for (unsigned j = 0; j < 6; ++j) a_piece(P, R, v0, v1, w, 26 + j, j);
      } else __builtin_amdgcn_s_sleep(S3SLEEP);
      v4u ga, gb;
      _Pragma("unroll 1") for (unsigned spin = 0; spin < 0x10000; ++spin) {
        ga = gld16<SC1>(rWS, (l & 15) * 16 + g * 2048, g3); gb = gld16<SC1>(rWS, (l & 15) * 16 + g * 2048 + 1024, g3);
        if (__all(ga.y == tg && ga.w == tg && gb.y == tg && gb.w == tg)) break;
        asm volatile("" ::: "memory");
      }
      STAMP(9 + 14 * pair);
      const float t1 = sum32(sum16(asf(ga.x) + asf(gb.x)));
      const float t2 = sum32(sum16(asf(ga.z) + asf(gb.z)));
      const float mean = t1 * (1.f / 448.f), var = fmaxf(t2 * (1.f / 448.f) - mean * mean, 0.f);
      const float rstd = __builtin_amdgcn_rsqf(var + 1e-5f), nb = -mean * rstd;
      const unsigned img = L_TOP + w * 2048;
      UNROLL for (int a = 0; a < 4; ++a)
        sts8(img + (l & 15) * 128 + 8 * ((4 * a + g) ^ (l & 15)),
             v2u{pk2(__builtin_fmaf(acc[a][0], rstd, nb), __builtin_fmaf(acc[a][1], rstd, nb)), pk2(__builtin_fmaf(acc[a][2], rstd, nb), __builtin_fmaf(acc[a][3], rstd, nb))});
      UNROLL for (int n = 0; n < 2; ++n) {
        const unsigned T = 4 * n + g, o = T & 1, QS = T >> 1, t0 = 8 * o + q4;
        const v4u d = tr16(img + t0 * 128 + 8 * ((4 * QS + (l & 3)) ^ t0), img + (t0 + 4) * 128 + 8 * ((4 * QS + (l & 3)) ^ (t0 + 4)));
        const unsigned quad = 4 * QS + q4, cq = 56 * (R.b_j & 1) + 4 * quad;
        const unsigned tile = cq < 96 ? 2 * (cq >> 5) + ((cq >> 2) & 1) : 6, ii = (cq < 96 ? 4 * ((cq & 31) >> 3) : cq - 96) + (l & 3);
        gst16<SC1>(d, rWS, quad < 14 ? tile * 1024 + ((2 * (w & 1) + o) * 16 + ii) * 16 : OOB,
                   WS_XN + pair * 2 * MiB + (R.b_j >> 1) * 458752 + (2 * R.b_q + (w >> 1)) * 7168);
      }
      if (pair == 0) a_read<26, 32>(R, l);
      DRAIN();
      BAR();
      if (w == 1) post(rWS, WS_MB + 2 * 131072 + (l * 4 + (R.b_j >> 1)) * 512 + (2 * R.b_q + (R.b_j & 1)) * 8, tag0 + 3);
      stage_h(P, R, l);
      if (pair == 0) {
        const unsigned t = l & 15, ff = (t >> 1) & 7, wn = L_T + w * 12288;
        const unsigned b0 = wn + t * 128 + 16 * (g ^ ff), b1 = wn + t * 128 + 16 * ((4 + g) ^ ff), lin = wn + l * 16;
        UNROLL for (int u = 0; u < 2; ++u) {
          UNROLL for (int mu = 0; mu < 4; ++mu) { A_LDS_WR(8 * u + mu, lin, mu * 2048); A_LDS_WR(8 * u + 4 + mu, lin, mu * 2048 + 1024); }
          LGKM0();
          UNROLL for (int mu = 0; mu < 4; ++mu) { A_LDS_RD(8 * u + mu, b0, mu * 2048); A_LDS_RD(8 * u + 4 + mu, b1, mu * 2048); }
          LGKM0();
        }
      }
      if (w == 0) poll(rWS, WS_MB + 2 * 131072 + (R.d_t * 4 + R.d_j) * 512 + l * 8, tag0 + 3);
      BAR();
    }
    STAMP(10 + 14 * pair);
    {
      const Role R(c0, w0);
      const unsigned l = opq(lane) & 63, w = R.w;
      f32x4 acc[4][7];
      v4u b[4][7];
      const unsigned xb = WS_XN + pair * 2 * MiB + R.d_j * 458752 + 16 * w * 7168;
      auto ldb = [&](int s, int n) { b[s & 3][n] = gld16<B1AUX>(rWS, l * 16 + (n & 3) * 1024, xb + ((s + R.rot) & 15) * 7168 + (n >> 2) * 4096); };
      UNROLL for (int s = 0; s < 3; ++s) UNROLL for (int n = 0; n < 7; ++n) { ldb(s, n); SB(); }
      UNROLL for (int s = 0; s < 16; ++s)
        UNROLL for (int n = 0; n < 7; ++n) {
          UNROLL for (int m = 0; m < 4; ++m) {
            if (s == 0) { MF16Az(acc[m][n], b[0][n], m); } else { MF16A(acc[m][n], b[s & 3][n], 4 * s + m); }
            if (m == 0 && s + 3 < 16) ldb(s + 3, n);
          }
          SB();
        }
      asm volatile("s_nop 7");
      PIN7(acc[0]); PIN7(acc[1]); PIN7(acc[2]); PIN7(acc[3]);
      STAMP(11 + 14 * pair);
      const unsigned r1 = opq(L_T + w * 14336 + l * 16), r2 = opq(L_T + (w ^ 1) * 14336 + l * 16), r3 = opq(L_T + (w ^ 3) * 14336 + l * 16);
      UNROLL for (int n = 0; n < 7; ++n) sts16(r1 + (7 + n) * 1024, __builtin_bit_cast(v4u, acc[3][n]));
      UNROLL for (int n = 0; n < 7; ++n) sts16(r1 + n * 1024, __builtin_bit_cast(v4u, acc[1][n]));
      BAR();
      UNROLL for (int n = 0; n < 7; ++n) acc[2][n] += __builtin_bit_cast(f32x4, lds16(r2 + (7 + n) * 1024));
      UNROLL for (int n = 0; n < 7; ++n) sts16(r2 + (7 + n) * 1024, __builtin_bit_cast(v4u, acc[2][n]));
      UNROLL for (int n = 0; n < 7; ++n) acc[0][n] += __builtin_bit_cast(f32x4, lds16(r2 + n * 1024));
      BAR();
      UNROLL for (int n = 0; n < 7; ++n) acc[0][n] += __builtin_bit_cast(f32x4, lds16(r3 + (7 + n) * 1024));
      const unsigned sg = (w >> 1) ? 0x80008000u : 0u;
      v2u o[7];
      UNROLL for (int n = 0; n < 7; ++n) o[n] = v2u{pk2(acc[0][n][0], acc[0][n][1]) ^ sg, pk2(acc[0][n][2], acc[0][n][3]) ^ sg};
      const unsigned so = WS_PQ + pair * 4 * MiB + (2 * R.d_t + (w & 1)) * 28672 + 7 * R.d_j * 1024;
      UNROLL for (int u = 0; u < 3; ++u) gst16<SC1>(cat(o[2 * u], o[2 * u + 1]), rWS, l * 16 + u * 1024, so + (w >> 1) * 3072);
      gst8<SC1>(o[6], rWS, l * 16, so + 6144 + (w >> 1) * 8);
      STAMP(12 + 14 * pair);
      DRAIN();
      BAR();
      if (w == 1 && l < 8) post(rWS, WS_MB + 3 * 131072 + ((R.d_t >> 1) * 8 + l) * 512 + (4 * (R.d_t & 1) + R.d_j) * 8, tag0 + 4);
      if (pair == 0) { const unsigned v = w_voff(l); FOR_3WAVES(28, n) { stage_w1(P, R, v, 1, n, L_T); stage_w1(P, R, v, 2, n, L_T + 28672); } }
    }
    {
      const Role R(c0, w0);
      const unsigned l = opq(lane) & 63, g = l >> 4, e16 = l & 15, f = (e16 >> 1) & 7, w = R.w;
      f32x4 acc[4];
      {
        constexpr int PRE = 5;
        unsigned hb[2][4], hm[2][4];
        UNROLL for (int k = 0; k < 4; ++k) {
          hb[0][k] = opq(((2 * k + g) >> 3) * 7168 + e16 * 128 + 16 * (((2 * k + g) & 7) ^ f)); hb[1][k] = opq(hb[0][k] + L_HI);
          hm[0][k] = opq(e16 * 128 + 16 * ((2 * k + (g >> 1)) ^ f) + 8 * (g & 1)); hm[1][k] = opq(hm[0][k] + L_HI);
        }
        v4u wf[PRE][4];
        auto rd = [&](int s, int a) {
          const int j = s / 7, i = s % 7, c = 14 * j + (i < 6 ? 4 * (i % 3) : 12);
          if (i < 6) wf[s % PRE][a] = lds16(hb[i / 3][(c & 7) >> 1] + (c >> 3) * 7168 + a * 2048);
          else wf[s % PRE][a] = cat(lds8(hm[0][(c & 7) >> 1] + (c >> 3) * 7168 + a * 2048), lds8(hm[1][(c & 7) >> 1] + (c >> 3) * 7168 + a * 2048));
        };
        UNROLL for (int s = 0; s < PRE; ++s) UNROLL for (int a = 0; a < 4; ++a) rd(s, a);
        UNROLL for (int a = 0; a < 4; ++a) acc[a] = f32x4{lo16(res[a].x), hi16(res[a].x), lo16(res[a].y), hi16(res[a].y)};
        asm volatile("" : "+v"(acc[0]), "+v"(acc[1]), "+v"(acc[2]), "+v"(acc[3]));
        if (w == 0 && l < 8) poll(rWS, WS_MB + 3 * 131072 + (R.b_q * 8 + R.b_j) * 512 + l * 8, tag0 + 4);
        BAR();
        STAMP(13 + 14 * pair);
        v4u pf[28];
        UNROLL for (int s = 0; s < 28; ++s) { pf[s] = gld16<SC1>(rWS, l * 16 + (s & 3) * 1024, WS_PQ + pair * 4 * MiB + (4 * R.b_q + w) * 28672 + (s >> 2) * 4096); SB(); }
        UNROLL for (int s = 0; s < 28; ++s)
          UNROLL for (int a = 0; a < 4; ++a) { MF16(acc[a], wf[s % PRE][a], pf[s]); if (s + PRE < 28) rd(s + PRE, a); SB(); }
        FENCE4(acc);
      }
      UNROLL for (int a = 0; a < 4; ++a) res[a] = v2u{pk2(acc[a][0], acc[a][1]), pk2(acc[a][2], acc[a][3])};
      if (pair == 1) {
        const rsrc_t rO = mk(P.out, 2048 * 448 * 2);
        const unsigned ro = (64 * R.b_q + 16 * w + e16) * 896 + (56 * R.b_j + 4 * g) * 2;
        UNROLL for (int a = 0; a < 4; ++a) gst8<0>(res[a], rO, (a == 3 && l >= 32) ? OOB : ro + 32 * a, 0);
        UNROLL for (int Js = 0; Js < 4; ++Js)
          asm volatile("" : "=v"(xf[Js][0]), "=v"(xf[Js][1]), "=v"(xf[Js][2]), "=v"(xf[Js][3]), "=v"(xf[Js][4]), "=v"(xf[Js][5]), "=v"(xf[Js][6]));
        asm volatile("" : "=v"(kT), "=v"(vv), "=v"(ssq));
      } else {
        const unsigned blk = 2 * R.b_q + (w >> 1);
        const unsigned so = WS_X2 + blk * 28672 + 7 * (R.b_j >> 1) * 1024 + (R.b_j & 1) * 512 + (w & 1) * 256;
        gst16<SC1>(cat(res[0], res[1]), rWS, g * 1024 + e16 * 16, so);
        gst16<SC1>(cat(res[2], res[3]), rWS, l < 32 ? (4 + g) * 1024 + e16 * 16 : OOB, so);
        gst8<SC1>(res[2], rWS, l < 32 ? OOB : 6 * 1024 + e16 * 16 + (g & 1) * 8, so);
        float s2 = 0.f, s3 = 0.f;
        UNROLL for (int a = 0; a < 3; ++a) UNROLL for (int e = 0; e < 4; ++e) s2 = __builtin_fmaf(acc[a][e], acc[a][e], s2);
        UNROLL for (int e = 0; e < 4; ++e) s3 = __builtin_fmaf(acc[3][e], acc[3][e], s3);
        s2 = sum32(sum16(s2 + (l < 32 ? s3 : 0.f)));
        __builtin_amdgcn_raw_buffer_store_b32(asu(s2), rWS, l < 16 ? l * 16 : OOB, WS_SSQ + blk * 1024 + (R.b_j >> 2) * 512 + (w & 1) * 256 + (R.b_j & 3) * 4, SC1);
        STAMP(14);
        DRAIN();
        BAR();
        if (w == 1 && l < 16) post(rWS, WS_MB + 4 * 131072 + ((R.b_q >> 1) * 16 + l) * 512 + (8 * (R.b_q & 1) + R.b_j) * 8, tag0 + 5);
        { const unsigned v = w_voff(l); FOR_3WAVES(28, n) stage_w1(P, R, v, 0, n, L_WQ); }
        patch_pad(R, l);
        if (w == 0 && l < 16) poll(rWS, WS_MB + 4 * 131072 + (R.a_p * 16 + R.a_h) * 512 + l * 8, tag0 + 5);
        BAR();
        STAMP(15);
        UNROLL for (int Js = 0; Js < 4; ++Js)
          UNROLL for (int i = 0; i < 7; ++i) {
            xf[Js][i] = gld16<SC1>(rWS, l * 16 + (i & 3) * 1024, WS_X2 + (4 * R.a_p + w) * 28672 + ((Js + w) & 3) * 7168 + (i >> 2) * 4096);
            SB();
          }
        const v4u sq = gld16<SC1>(rWS, l * 16, WS_SSQ + (4 * R.a_p + w) * 1024);
        SB();
        kv_gemm(l, w, [](int) {});
        ssq = (asf(sq.x) + asf(sq.y)) + (asf(sq.z) + asf(sq.w));
      }
    }
  }
  if (c0 == 0 && w0 == 0 && lane == 0) {
    const u64 nn = nonce + 16;
    gst8<SC1>(v2u{(unsigned)nn, (unsigned)(nn >> 32)}, rWS, 0, WS_NONCE);
  }
}

}

extern "C" void kernel_launch(void* const* d_in, const int* in_sizes, int n_in, void* d_out, int out_size, void* d_ws, size_t ws_size,
                              hipStream_t stream) {
  fused::Params p;
  p.x = d_in[0]; p.wqkv = d_in[1]; p.wphi = d_in[2]; p.wo = d_in[3];
  p.cr = d_in[5]; p.ci = d_in[6]; p.hr = d_in[7]; p.hi = d_in[8];
  p.out = d_out; p.ws = d_ws;
  fused::fused_fwd<<<256, 256, 0, stream>>>(p);
}
```

```cpp
#include <hip/hip_runtime.h>
#include <hip/hip_bf16.h>
#include <stdint.h>

namespace fused {

typedef unsigned v4u __attribute__((ext_vector_type(4)));
typedef unsigned v2u __attribute__((ext_vector_type(2)));
typedef float f32x2 __attribute__((ext_vector_type(2)));
typedef float f32x4 __attribute__((ext_vector_type(4)));
typedef float f32x16 __attribute__((ext_vector_type(16)));
typedef __bf16 bf16x2 __attribute__((ext_vector_type(2)));
typedef __bf16 bf16x8 __attribute__((ext_vector_type(8)));
typedef short s16x4 __attribute__((ext_vector_type(4)));
typedef __amdgpu_buffer_rsrc_t rsrc_t;
typedef unsigned long long u64;
#define LAS __attribute__((address_space(3)))
#define DI __device__ __forceinline__

constexpr unsigned MiB = 1u << 20;
constexpr unsigned WS_NONCE = 0;
constexpr unsigned WS_MB = 4096;
constexpr unsigned WS_G3 = 1 * MiB;
constexpr unsigned WS_KVP = 2 * MiB;
constexpr unsigned WS_OF = 4 * MiB;
constexpr unsigned WS_XN = 8 * MiB;
constexpr unsigned WS_PQ = 12 * MiB;
constexpr unsigned WS_X2 = 20 * MiB;
constexpr unsigned WS_SSQ = 22 * MiB;
constexpr unsigned WS_WPK = 22 * MiB + 262144;
constexpr unsigned WS_STAMP = 23 * MiB + 786432;
constexpr unsigned WS_BYTES = 24 * MiB;
constexpr unsigned OOB = 0x7FFFFFF0u;
constexpr int SC1 = 16;

constexpr unsigned L_WQ = 0, L_WO = 28672, L_MISC = 86016, L_SMALL = 102400, L_T = 106496, L_TOP = 155648;
constexpr unsigned L_WPHI = L_SMALL, L_CEX = L_SMALL + 1664, L_STAMP = L_SMALL + 3328, L_JUNK = L_SMALL + 3840;
constexpr unsigned L_HI = 50176;
constexpr unsigned LDS_BYTES = 163840;

struct Params {
  const void *x, *wqkv, *wphi, *wo, *cr, *ci, *hr, *hi;
  void *out, *ws;
};

#define MF16(c, a, b) asm volatile("v_mfma_f32_16x16x32_bf16 %0, %1, %2, %0" : "+v"(c) : "v"(a), "v"(b))
#define MF16z(c, a, b) asm volatile("v_mfma_f32_16x16x32_bf16 %0, %1, %2, 0" : "=&v"(c) : "v"(a), "v"(b))
#define nMF16(c, a, b) asm volatile("s_nop 1\n\tv_mfma_f32_16x16x32_bf16 %0, %1, %2, %0" : "+v"(c) : "v"(a), "v"(b))
#define MF32(c, a, b) asm volatile("v_mfma_f32_32x32x16_bf16 %0, %1, %2, %0" : "+v"(c) : "v"(a), "v"(b))
#define MF32z(c, a, b) asm volatile("v_mfma_f32_32x32x16_bf16 %0, %1, %2, 0" : "=&v"(c) : "v"(a), "v"(b))
#define nMF32(c, a, b) asm volatile("s_nop 1\n\tv_mfma_f32_32x32x16_bf16 %0, %1, %2, %0" : "+v"(c) : "v"(a), "v"(b))
#define nMF32z(c, a, b) asm volatile("s_nop 1\n\tv_mfma_f32_32x32x16_bf16 %0, %1, %2, 0" : "=&v"(c) : "v"(a), "v"(b))
#define MF16A(c, a, R) asm volatile("v_mfma_f32_16x16x32_bf16 %0, %1, a[%c2:%c3], %0" : "+v"(c) : "v"(a), "i"(4 * (R)), "i"(4 * (R) + 3))
#define MF16Az(c, a, R) asm volatile("v_mfma_f32_16x16x32_bf16 %0, %1, a[%c2:%c3], 0" : "=&v"(c) : "v"(a), "i"(4 * (R)), "i"(4 * (R) + 3))
#define FENCE1(c) asm volatile("s_nop 11" : "+v"(c))
#define FENCE2(c, d) asm volatile("s_nop 11" : "+v"(c), "+v"(d))
#define FENCE4(c) asm volatile("s_nop 7" : "+v"(c[0]), "+v"(c[1]), "+v"(c[2]), "+v"(c[3]))
#define PIN7(c) asm volatile("" : "+v"(c[0]), "+v"(c[1]), "+v"(c[2]), "+v"(c[3]), "+v"(c[4]), "+v"(c[5]), "+v"(c[6]))
#define A_LDS_RD(R, addr, off) asm volatile("ds_read_b128 a[%c0:%c1], %2 offset:%c3" ::"i"(4 * (R)), "i"(4 * (R) + 3), "v"(addr), "i"(off) : "memory")
DI float asf(unsigned u) { return __builtin_bit_cast(float, u); }
DI unsigned asu(float f) { return __builtin_bit_cast(unsigned, f); }
DI unsigned pk2(float a, float b) { return __builtin_bit_cast(unsigned, __builtin_convertvector(f32x2{a, b}, bf16x2)); }
DI float lo16(unsigned u) { return asf(u << 16); }
DI float hi16(unsigned u) { return asf(u & 0xffff0000u); }
DI float dot2(unsigned a, unsigned b, float c) { return __builtin_amdgcn_fdot2_f32_bf16(__builtin_bit_cast(bf16x2, a), __builtin_bit_cast(bf16x2, b), c, false); }
DI v4u lds16(unsigned a) { return *(LAS const v4u*)(size_t)a; }
DI v2u lds8(unsigned a) { return *(LAS const v2u*)(size_t)a; }
DI float lds4(unsigned a) { return *(LAS const float*)(size_t)a; }
DI void sts16(unsigned a, v4u v) { *(LAS v4u*)(size_t)a = v; }
DI void sts8(unsigned a, v2u v) { *(LAS v2u*)(size_t)a = v; }
DI void sts4(unsigned a, float v) { *(LAS float*)(size_t)a = v; }
DI v2u tr8(unsigned a) { return __builtin_bit_cast(v2u, __builtin_amdgcn_ds_read_tr16_b64_v4i16((LAS s16x4*)(size_t)a)); }
DI v4u cat(v2u a, v2u b) { return v4u{a.x, a.y, b.x, b.y}; }
DI v4u tr16(unsigned a0, unsigned a1) { return cat(tr8(a0), tr8(a1)); }
template <int AUX> DI v4u gld16(rsrc_t r, unsigned v, unsigned s) { return __builtin_amdgcn_raw_buffer_load_b128(r, v, s, AUX); }
template <int AUX> DI v2u gld8(rsrc_t r, unsigned v, unsigned s) { return __builtin_amdgcn_raw_buffer_load_b64(r, v, s, AUX); }
template <int AUX> DI void gst16(v4u d, rsrc_t r, unsigned v, unsigned s) { __builtin_amdgcn_raw_buffer_store_b128(d, r, v, s, AUX); }
template <int AUX> DI void gst8(v2u d, rsrc_t r, unsigned v, unsigned s) { __builtin_amdgcn_raw_buffer_store_b64(d, r, v, s, AUX); }
DI rsrc_t mk(const void* p, unsigned bytes) { return __builtin_amdgcn_make_buffer_rsrc((void*)p, 0, bytes, 0x00020000); }
DI void dma16c(rsrc_t r, unsigned v, unsigned s, unsigned lds) {
  asm volatile("s_mov_b32 m0, %3\n\ts_nop 0\n\tbuffer_load_dwordx4 %0, %1, %2 offen sc1 lds" ::"v"(v), "s"(r), "s"(s), "s"(lds) : "memory");
}
DI void dma16(rsrc_t r, unsigned v, unsigned s, unsigned lds) {
  asm volatile("s_mov_b32 m0, %3\n\ts_nop 0\n\tbuffer_load_dwordx4 %0, %1, %2 offen lds" ::"v"(v), "s"(r), "s"(s), "s"(lds) : "memory");
}
DI void touch(const void* base, unsigned off) {
  asm volatile("s_mov_b32 m0, %1\n\ts_nop 0\n\tglobal_load_lds_dword %0, off" ::"v"((const char*)base + off), "s"(L_JUNK) : "memory");
}
#define VMC(n) asm volatile("s_waitcnt vmcnt(%c0)" ::"i"(n) : "memory")
#define BAR() asm volatile("s_waitcnt lgkmcnt(0)\n\ts_barrier" ::: "memory")
#define DRAIN() asm volatile("s_waitcnt vmcnt(0)" ::: "memory")
#define LGKM0() asm volatile("s_waitcnt lgkmcnt(0)" ::: "memory")
#define SB() __builtin_amdgcn_sched_barrier(0)
#define UNROLL _Pragma("unroll")
DI unsigned opq(unsigned v) { asm volatile("" : "+v"(v)); return v; }
DI unsigned sopq(unsigned v) { asm volatile("" : "+s"(v)); return v; }
DI float sum32(float v) { auto r = __builtin_amdgcn_permlane32_swap(asu(v), asu(v), false, false); return asf(r[0]) + asf(r[1]); }
DI float sum16(float v) { auto r = __builtin_amdgcn_permlane16_swap(asu(v), asu(v), false, false); return asf(r[0]) + asf(r[1]); }
DI float max32(float v) { auto r = __builtin_amdgcn_permlane32_swap(asu(v), asu(v), false, false); return fmaxf(asf(r[0]), asf(r[1])); }
DI float min32(float v) { auto r = __builtin_amdgcn_permlane32_swap(asu(v), asu(v), false, false); return fminf(asf(r[0]), asf(r[1])); }

DI void post(rsrc_t ws, unsigned voff, u64 tag) {
  asm volatile("buffer_atomic_swap_x2 %0, %1, %2, 0 offen\n\ts_nop 1" ::"v"(tag), "v"(voff), "s"(ws) : "memory");
}
DI void poll(rsrc_t ws, unsigned voff, u64 tag) {
  unsigned cnt = 0; u64 t;
  asm volatile(
      "Lp%=:\n\tv_mov_b64 %1, 0\n\ts_nop 1\n\tbuffer_atomic_add_x2 %1, %2, %3, 0 offen sc0\n\ts_add_u32 %0, %0, 1\n\ts_waitcnt vmcnt(0)\n\t"
      "v_cmp_eq_u64 vcc, %4, %1\n\ts_nop 1\n\ts_cmp_eq_u64 vcc, exec\n\ts_cbranch_scc1 Ld%=\n\t"
      "s_cmp_lt_u32 %0, 0x10000\n\ts_cbranch_scc1 Lp%=\n\tLd%=:"
      : "+s"(cnt), "=&v"(t) : "v"(voff), "s"(ws), "s"(tag) : "vcc", "scc", "memory");
}

#define CSTAMP(i) do {} while (0)
#define STAMP(i) do {} while (0)

DI constexpr int rhoA(int i, int t) { return i < 4 ? (t ? 16 + 4 * i : 4 * i) : (i < 6 ? (t ? 48 + 4 * (i - 4) : 32 + 4 * (i - 4)) : (t ? 44 : 40)); }
DI constexpr int rhoO(int i, int t) { return i == 0 ? (t ? 8 : 0) : i == 1 ? (t ? 24 : 16) : i == 2 ? (t ? 12 : 4) : i == 3 ? (t ? 32 : 20) : i == 4 ? (t ? 36 : 28) : i == 5 ? (t ? 52 : 44) : (t ? 48 : 40); }

DI v4u pack8(const f32x16& x, int s) { return v4u{pk2(x[8 * s], x[8 * s + 1]), pk2(x[8 * s + 2], x[8 * s + 3]), pk2(x[8 * s + 4], x[8 * s + 5]), pk2(x[8 * s + 6], x[8 * s + 7])}; }

DI void dual_exp(const f32x16& p, bool h, float sc, float (&ep)[16], float (&en)[16], float& sp, float& sn) {
  float q[16];
  UNROLL for (int i = 0; i < 16; ++i) q[i] = (i >= 12 && h) ? p[0] : p[i];
  float mx = q[0], mn = q[0];
  UNROLL for (int i = 1; i < 16; ++i) { mx = fmaxf(mx, q[i]); mn = fminf(mn, q[i]); }
  mx = max32(mx); mn = min32(mn);
  const float a = -mx * sc, b = mn * sc;
  sp = 0.f; sn = 0.f;
  UNROLL for (int i = 0; i < 16; ++i) {
    float e = __builtin_amdgcn_exp2f(__builtin_fmaf(q[i], sc, a)), f = __builtin_amdgcn_exp2f(__builtin_fmaf(q[i], -sc, b));
    if (i >= 12) { e = h ? 0.f : e; f = h ? 0.f : f; }
    ep[i] = e; en[i] = f; sp += e; sn += f;
  }
  sp = sum32(sp); sn = sum32(sn);
}

#define S3SLEEP 4
#define QMID 17
#define B1GR 8
#define B1DIST 14
#define PRETOUCH 1
struct Role {
  unsigned w, xcd, rr, a_p, a_h, par, j0, b_q, b_j, d_t, d_j, rot;
  DI Role(unsigned c0, unsigned w0) {
    const unsigned c = sopq(c0);
    xcd = c & 7; rr = c >> 3; w = sopq(w0);
    a_p = 4 * (xcd >> 1) + (rr >> 3); a_h = 8 * (xcd & 1) + (rr & 7); par = a_h & 1;
    j0 = (w ^ a_h) & 3;
    b_q = 4 * xcd + (rr >> 3); b_j = rr & 7;
    d_t = 8 * xcd + (rr >> 2); d_j = rr & 3;
    rot = B1GR * (d_t & 1);
  }
};

DI unsigned w_voff(unsigned l) { return (l >> 2) * 2688 + (l & 3) * 16; }
DI void stage_w1(const Params& P, const Role& R, unsigned v, unsigned part, unsigned n, unsigned dst) {
  dma16(mk(P.wqkv, 448 * 1344 * 2), v, n * (16 * 2688) + part * 896 + ((56 * R.a_h) & ~15u), dst + n * 1024);
}
#define FOR_3WAVES(N, n) _Pragma("unroll 1") for (unsigned n = R.w ? R.w - 1 : (N); n < (N); n += 3)
DI void patch_pad(const Role& R, unsigned l) {
  const unsigned a = L_T + (112 * R.w + l) * 64 + (R.par ? 0 : 56);
  sts8(a, v2u{0x00003f80u, 0u}); sts8(a + 28672, v2u{0u, 0u});
  if (l < 48) { sts8(a + 4096, v2u{0x00003f80u, 0u}); sts8(a + 4096 + 28672, v2u{0u, 0u}); }
}
DI unsigned wo_voff(unsigned l, int hf) { return (l >> 3) * 896 + 16 * (2 * (((l & 7) >> 1) ^ (((l >> 4) & 1) | (hf << 1))) + (l & 1)); }
DI void stage_h(const Params& P, const Role& R, unsigned l) {
  const unsigned v0 = (l >> 3) * 896 + 16 * ((l & 7) ^ (l >> 4)), v1 = (l >> 3) * 896 + 16 * ((l & 7) ^ (4 + (l >> 4)));
  FOR_3WAVES(14, n) {
    const unsigned m = n >= 7, u = n - 7 * m;
    const rsrc_t rH = mk(m ? P.hi : P.hr, 448 * 448 * 2);
    UNROLL for (int o = 0; o < 7; ++o) dma16(rH, (o & 1) ? v1 : v0, (56 * R.b_j + 8 * o) * 896 + u * 128, n * 7168 + o * 1024);
  }
}
DI unsigned a_voff(unsigned l, int o) { return (8 * o + (l >> 3)) * 4096 + 16 * ((l & 7) ^ ((4 * o + (l >> 4)) & 7)); }
DI void a_piece(const Params& P, const Role& R, unsigned v0, unsigned v1, unsigned w, unsigned n, unsigned j) {
  const rsrc_t r = mk(((n ^ w) & 2) ? P.ci : P.cr, 2048 * 2048 * 2);
  const unsigned so = (32 * R.d_t + 16 * ((n ^ w) & 1)) * 4096 + 1024 * w + 128 * (((n >> 2) + (R.rot >> 1)) & 7);
  const unsigned dst = j < 6 ? L_T + w * 12288 + j * 2048 : L_WQ + w * 6144 + (j - 6) * 2048;
  dma16(r, v0, so, dst); dma16(r, v1, so, dst + 1024);
}
DI void a_issue_all(const Params& P, const Role& R, unsigned l, unsigned n0) {
  const unsigned v0 = a_voff(l, 0), v1 = a_voff(l, 1);
  FOR_3WAVES(36, i) a_piece(P, R, v0, v1, i & 3, n0 + (i >> 2), i >> 2);
}
DI void a_issue_own(const Params& P, const Role& R, unsigned l, unsigned n0, unsigned cnt) {
  const unsigned v0 = a_voff(l, 0), v1 = a_voff(l, 1);
  _Pragma("unroll 1") for (unsigned j = 0; j < cnt; ++j) a_piece(P, R, v0, v1, R.w, n0 + j, j);
}
template <int N0, int N1> DI void a_read(const Role& R, unsigned l) {
  const unsigned t = l & 15, f = (t >> 1) & 7, b0 = t * 128 + 16 * ((l >> 4) ^ f), b1 = t * 128 + 16 * ((4 + (l >> 4)) ^ f);
  const unsigned x0 = b0 + L_T + R.w * 12288, x1 = b1 + L_T + R.w * 12288, y0 = b0 + L_WQ + R.w * 6144, y1 = b1 + L_WQ + R.w * 6144;
  UNROLL for (int n = N0; n < N1; ++n) {
    const int j = n - N0, r = 8 * (n >> 2) + (n & 3);
    if (j < 6) { A_LDS_RD(r, x0, j * 2048); A_LDS_RD(r + 4, x1, j * 2048); }
    else { A_LDS_RD(r, y0, (j - 6) * 2048); A_LDS_RD(r + 4, y1, (j - 6) * 2048); }
  }
  LGKM0();
}
DI void a_touch(const Params& P, const Role& R, unsigned l, unsigned u0) {
  touch((R.d_j >> 1) ? P.ci : P.cr, (32 * R.d_t + 16 * (R.d_j & 1) + (l >> 2)) * 4096 + 1024 * R.w + 128 * ((u0 + (l & 3) + (R.rot >> 1)) & 7));
}
DI void ws_touch(const Params& P, const Role& R, unsigned i, unsigned pair) {
  const unsigned Pp = R.a_h >> 1;
  const unsigned o1 = WS_XN + pair * 2 * MiB + (R.b_j >> 1) * 458752 + (2 * R.b_q + (R.b_j & 1)) * 7168 + i * 128;
  const unsigned o2 = WS_OF + pair * 2 * MiB + (8 * R.a_p + (i & 7)) * 14336 + (7 * (Pp >> 2) + 3) * 1024 + (Pp & 3) * 256 + R.par * 128;
  const unsigned o3 = WS_G3 + pair * 262144 + R.b_q * 8192 + R.b_j * 1024 + (i & 7) * 128;
  touch(P.ws, i < 56 ? o1 : (i < 64 ? o2 : o3));
}
DI unsigned colsum_all(unsigned l, int part) {
  const unsigned cx = opq(L_CEX + (l & 31) * 4);
  float s = 0.f;
  UNROLL for (int i = 0; i < 4; ++i) s += lds4(cx + (i * 3 + part) * 128);
  return pk2(s, s - lo16(pk2(s, s)));
}

__global__ void __launch_bounds__(256, 1) fused_fwd(Params P) {
  __shared__ __attribute__((aligned(1024))) char smem[LDS_BYTES];
  asm volatile("" ::"v"((unsigned)(size_t)(LAS char*)smem) : "memory", "a255");
  const char* code; unsigned code_len;
  asm volatile("s_getpc_b64 %0\nLfused_begin:\n\ts_mov_b32 %1, Lfused_end-Lfused_begin" : "=s"(code), "=s"(code_len));
  const unsigned lane = threadIdx.x & 63;
  const unsigned w0 = __builtin_amdgcn_readfirstlane(threadIdx.x >> 6), c0 = blockIdx.x;
  const rsrc_t rWS = mk(P.ws, WS_BYTES);
  STAMP(0); CSTAMP(29);

  v2u res[4];
  v4u xf[4][7];
  f32x16 kT, vv;
  float ssq;
  unsigned csk, csv, csq = 0;
  const u64 nonce = *(const __attribute__((address_space(4))) u64*)P.ws;
  auto kv_gemm = [&](unsigned l, unsigned j0, auto&& hook) {
    const unsigned wl = L_T + (56 * (l >> 5) + ((l & 15) >> 2)) * 64 + ((l >> 4) & 1) * 32 + (l & 3) * 8;
    v4u wk[3], wv[3];
    unsigned a;
    auto rd = [&](int s) {
      if (s % 7 == 0) a = opq(wl + ((s / 7) ^ j0) * 7168);
      wk[s % 3] = tr16(a + rhoA(s % 7, 0) * 64, a + rhoA(s % 7, 1) * 64); wv[s % 3] = tr16(a + 28672 + rhoA(s % 7, 0) * 64, a + 28672 + rhoA(s % 7, 1) * 64);
    };
    rd(0); rd(1);
    SB();
    UNROLL for (int s = 0; s < 28; ++s) {
      const v4u x = xf[s / 7][s % 7], k = wk[s % 3], b = wv[s % 3];
      if (s == 0) { MF32z(kT, k, x); } else { MF32(kT, k, x); }
      if (s + 2 < 28) rd(s + 2);
      SB();
      if (s == 0) { MF32z(vv, x, b); } else { MF32(vv, x, b); }
      hook(s, k, b);
      SB();
    }
  };

  {
    const Role R(c0, w0);
    const unsigned l = opq(lane) & 63, w = R.w, j0 = R.j0;
    {
      const unsigned i = 64 * w + l, j = i - 112, r12 = (j * 5462) >> 16, k12 = j - 12 * r12;
      const unsigned ox = (R.xcd >> 1) * (512 * 896) + (112 * R.rr + i) * 128;
      const unsigned ow = (14 * R.rr + r12) * 2688 + (k12 >> 2) * 896 + ((R.xcd & 1) * 3 + (k12 & 3)) * 128;
      touch(i < 112 ? P.x : P.wqkv, i < 112 ? ox : ow);
      const unsigned i2 = 16 * w + l, j2 = i2 + 144, r2 = (j2 * 5462) >> 16, k2 = j2 - 12 * r2;
      const unsigned ow2 = (14 * R.rr + r2) * 2688 + (k2 >> 2) * 896 + ((R.xcd & 1) * 3 + (k2 & 3)) * 128;
      const unsigned op = (R.a_h * 1568 + (i2 - 24) * 128) & ~3u, oc = (R.rr * 12 + i2 - 37) * 128;
      if (l < 16 && i2 < 49) touch(i2 < 24 ? P.wqkv : (i2 < 37 ? P.wphi : code), i2 < 24 ? ow2 : (i2 < 37 ? op : (oc < code_len ? oc : 0)));
      if (l < 2) {
        const unsigned k = 2 * w + l, kind = k < 2 ? k : (k < 6 ? 2 : k - 3);
        const unsigned id = (kind == 0 || kind == 4) ? R.a_p * 16 + R.a_h : (kind == 2 ? R.d_t * 4 + R.d_j : R.b_q * 8 + R.b_j);
        asm volatile("buffer_atomic_add %0, %1, %2, 0 offen" ::"v"(0u), "v"(WS_MB + kind * 131072 + id * 512 + (kind == 2 ? (k - 2) * 128 : 0)), "s"(rWS) : "memory");
      }
    }
    const rsrc_t rX = mk(P.x, 2048 * 448 * 2);
    const unsigned wv0 = w_voff(l);
    _Pragma("unroll 1") for (unsigned n = 7 * w; n < 7 * w + 7; ++n) { stage_w1(P, R, wv0, 1, n, L_T); stage_w1(P, R, wv0, 2, n, L_T + 28672); }
    const unsigned win = w * 16384, xs = (128 * R.a_p + 32 * w) * 896, xc = 16 * ((l & 31) ^ (l >> 5)), xr = (l >> 5) * 896;
    auto x_issue = [&](unsigned hf) { _Pragma("unroll 2") for (unsigned n = 0; n < 16; ++n) dma16(rX, (xc ^ ((32 * n) & 0xf0)) + xr, xs + 384 * hf + n * 1792, win + n * 1024); };
    x_issue(j0 >> 1);
    const unsigned xb = opq(win + (l & 31) * 512), sw = (l & 15) << 4;
    auto x_read = [&](int Js, int i) {
      const unsigned J = j0 ^ Js, o = 224 * J - 384 * (J >> 1) + 112 * (l >> 5);
      xf[Js][i] = cat(lds8(xb + ((o + 2 * rhoA(i, 0)) ^ sw)), lds8(xb + ((o + 2 * rhoA(i, 1)) ^ sw)));
    };
    float q0 = 0.f, q1 = 0.f, q2 = 0.f, q3 = 0.f;
    auto sq = [&](int Js) { UNROLL for (int i = 0; i < 7; ++i) { const v4u x = xf[Js][i]; q0 = dot2(x.x, x.x, q0); q1 = dot2(x.y, x.y, q1); q2 = dot2(x.z, x.z, q2); q3 = dot2(x.w, x.w, q3); } };
    STAMP(31);
    VMC(16);
    patch_pad(R, l);
    VMC(0);
    STAMP(32);
    UNROLL for (int i = 0; i < 14; ++i) x_read(i / 7, i % 7);
    LGKM0();
    x_issue((j0 >> 1) ^ 1);
    sq(0); sq(1);
    asm volatile("" : "+v"(q0), "+v"(q1), "+v"(q2), "+v"(q3));
    BAR();
    STAMP(1);
    const rsrc_t rP = mk(P.wphi, 16 * 784 * 2);
    const unsigned ro = (64 * R.b_q + 16 * w + (l & 15)) * 896 + (56 * R.b_j + 4 * (l >> 4)) * 2;
    const v4u ones = v4u{0x3f803f80u, 0x3f803f80u, 0x3f803f80u, 0x3f803f80u};
    f32x16 ck, cv;
    v4u pk0, pk1;
    kv_gemm(l, j0, [&](int s, const v4u& k, const v4u& b) {
      if (s == 0) { MF32z(ck, ones, k); MF32z(cv, ones, b); } else if (s < 7) { MF32(ck, ones, k); MF32(cv, ones, b); }
      if (s == 8) asm volatile("" : "+v"(ck), "+v"(cv));
      if (s == 6) VMC(0);
      if (s >= 6 && s < 13) { x_read(2, s - 6); x_read(3, s - 6); }
      if (s == 13) {
        sts4(L_CEX + (w * 3 + (l >> 5)) * 128 + (l & 31) * 4, (l >> 5) ? cv[0] : ck[0]);
        const unsigned a = L_T + (w & 1) * 28672 + R.a_p * 1792 + l * 16;
        pk0 = lds16(a); pk1 = lds16(a + 1024);
        BAR();
      }
      if (s >= 13 && s < 20) stage_w1(P, R, wv0, 0, 7 * w + s - 13, L_WQ);
      else if (s == 20) { if ((w & 1) * 64 + l < 98) dma16(rP, ((w & 1) * 64 + l) * 16, R.a_h * 1568, L_WPHI + (w & 1) * 1024); }
      else if (s > 20 && s < 25) asm volatile("buffer_load_dwordx2 %0, %1, %2, 0 offen" : "=&v"(res[s - 21]) : "v"((s == 24 && l >= 32) ? OOB : ro + 32 * (s - 21)), "s"(rX) : "memory");
      else if (s == 25) {
        if (w < 2) { if (l < 49) touch(P.wo, (98 * R.rr + 49 * w + l) * 128); }
        else if (l < 36) ws_touch(P, R, PRETOUCH ? 36 * (w - 2) + l : 0, 0);
      } else if (s == 26) a_touch(P, R, l, 0);
      else if (s == 27) gst16<SC1>((w >> 1) ? pk1 : pk0, rWS, (w >> 1) && l >= 48 ? OOB : l * 16, WS_WPK + R.a_h * 86016 + (w & 1) * 28672 + R.a_p * 1792 + (w >> 1) * 1024);
    });
    sq(2); sq(3);
    ssq = (q0 + q1) + (q2 + q3);
    csk = colsum_all(l, 0); csv = colsum_all(l, 1);
    FENCE2(kT, vv);
    asm volatile("s_waitcnt vmcnt(3)" : "+v"(res[0]), "+v"(res[1]), "+v"(res[2]), "+v"(res[3])::"memory");
  }

  _Pragma("unroll 1") for (unsigned pair = 0; pair < 2; ++pair) {
    const u64 tag0 = nonce + 1 + 6 * pair;
    v4u phq[4]; float spq;
    {
      const Role R(c0, w0);
      const unsigned l = opq(lane) & 63, h = l >> 5, w = R.w, par = R.par;
      STAMP(2 + 14 * pair);
      float mean;
      {
        const float sx = par ? kT[0] : kT[12];
        auto r = __builtin_amdgcn_permlane32_swap(asu(sx), asu(sx), false, false);
        mean = asf(par ? r[0] : r[1]) * (1.f / 448.f);
      }
      const float var = fmaxf(sum32(ssq) * (1.f / 448.f) - mean * mean, 0.f);
      const float sig = __builtin_amdgcn_sqrtf(var + 1e-5f), rstd = __builtin_amdgcn_rcpf(sig);
      v4u xa;
      {
        const unsigned mh = pk2(-mean, -mean), ml = pk2(-mean - lo16(mh), -mean - lo16(mh));
        const unsigned sh = pk2(sig, sig), sl = pk2(sig, sig - lo16(sh));
        xa = v4u{h ? 0u : mh, h ? 0u : ml, h ? 0u : sl, 0u};
      }
      {
        const unsigned pd = par ? 0 : 28;
        const unsigned one = (l & 31) == pd ? 0x00003f80u : ((l & 31) == pd + 1 ? 0x3f800000u : 0u);
        const v4u a1 = v4u{csk, csk, 0u, 0u}, a2 = v4u{csv, csv, one, 0u};
        nMF32(kT, a1, xa); nMF32(vv, xa, a2);
      }
      BAR();
      v4u wp[2];
      {
        const int q4 = (l & 15) >> 2, fq = 4 * ((l >> 4) & 1) + (l & 3);
        UNROLL for (int s = 0; s < 2; ++s) {
          v2u t2[2];
          UNROLL for (int t = 0; t < 2; ++t) {
            const int d0 = 16 * s + 8 * t + 4 * (int)h - 4 * (int)par;
            const bool ok = d0 >= 0 && d0 < 28;
            const v2u r = tr8(L_WPHI + ((ok ? d0 : 0) + q4) * 56 + (fq < 7 ? fq : 0) * 8);
            const bool keep = ok && (l & 31) < 28;
            t2[t] = v2u{keep ? r.x : 0u, keep ? r.y : 0u};
          }
          wp[s] = cat(t2[0], t2[1]);
        }
      }
      const float sc = rstd * 1.44269504088896f;
      {
        FENCE2(kT, vv);
        f32x16 pk;
        const v4u k0 = pack8(kT, 0), k1 = pack8(kT, 1);
        nMF32z(pk, wp[0], k0); nMF32(pk, wp[1], k1);
        sts16(L_TOP + w * 2048 + l * 16, pack8(vv, 0)); sts16(L_TOP + w * 2048 + 1024 + l * 16, pack8(vv, 1));
        FENCE1(pk);
        float ep[16], en[16], sp, sn;
        dual_exp(pk, h, sc, ep, en, sp, sn);
        const float fp = rstd * __builtin_amdgcn_rcpf(sp), fn = rstd * __builtin_amdgcn_rcpf(sn);
        const unsigned tok = l & 31, sz = (tok & 7) | ((((tok >> 1) ^ (tok >> 3)) & 1) << 3), pb = L_MISC + w * 4096 + tok * 128;
        UNROLL for (int G = 0; G < 4; ++G) {
          sts8(pb + 8 * ((2 * G + h) ^ sz), v2u{pk2(ep[4 * G] * fp, ep[4 * G + 1] * fp), pk2(ep[4 * G + 2] * fp, ep[4 * G + 3] * fp)});
          sts8(pb + 8 * ((8 + 2 * G + h) ^ sz), v2u{pk2(en[4 * G] * fn, en[4 * G + 1] * fn), pk2(en[4 * G + 2] * fn, en[4 * G + 3] * fn)});
        }
      }
      BAR();
      {
        const unsigned g = l >> 4, q4 = (l & 15) >> 2, U = (w >> 1) * 8 + 4 * (w & 1) + (l & 3);
        const unsigned t0 = 16 * (g >> 1) + 4 * (g & 1) + q4, lo3 = 4 * (g & 1) + q4;
        const unsigned p0 = opq(L_MISC + t0 * 128 + 8 * (U ^ (lo3 | (((q4 >> 1) & 1) << 3)))), p1 = opq(L_MISC + (t0 + 8) * 128 + 8 * (U ^ (lo3 | ((((q4 >> 1) & 1) ^ 1) << 3))));
        const unsigned vb = opq(L_TOP + (g >> 1) * 1024 + ((g & 1) * 32 + (l & 15)) * 16);
        v4u a[4], b0[4], b1[4];
        UNROLL for (int i = 0; i < 4; ++i) { a[i] = tr16(p0 + i * 4096, p1 + i * 4096); b0[i] = lds16(vb + i * 2048); b1[i] = lds16(vb + i * 2048 + 256); }
        f32x4 d0, d1;
        MF16z(d0, a[0], b0[0]); MF16z(d1, a[0], b1[0]);
        UNROLL for (int i = 1; i < 4; ++i) { MF16(d0, a[i], b0[i]); MF16(d1, a[i], b1[i]); }
        asm volatile("s_nop 7" : "+v"(d0), "+v"(d1));
        auto r0 = __builtin_amdgcn_permlane32_swap(pk2(d0[0], d0[1]), pk2(d1[0], d1[1]), false, false);
        auto r1 = __builtin_amdgcn_permlane32_swap(pk2(d0[2], d0[3]), pk2(d1[2], d1[3]), false, false);
        gst16<SC1>(v4u{r0[0], r1[0], r0[1], r1[1]}, rWS, ((g & 1) * 32 + (l >> 5) * 16 + (l & 15)) * 16, WS_KVP + pair * MiB + (R.a_h * 16 + R.a_p) * 4096 + w * 1024);
      }
      STAMP(3 + 14 * pair);
      {
        f32x16 qT;
        v4u wq[5];
        unsigned a;
        const unsigned wl = L_WQ + (56 * h + ((l & 15) >> 2)) * 64 + ((l >> 4) & 1) * 32 + (l & 3) * 8;
        auto rd = [&](int s) { if (s % 7 == 0) a = opq(wl + ((s / 7) ^ R.j0) * 7168); wq[s % 5] = tr16(a + rhoA(s % 7, 0) * 64, a + rhoA(s % 7, 1) * 64); };
        const rsrc_t rWO = mk(P.wo, 448 * 448 * 2);
        const unsigned ov0 = wo_voff(l, 0), ov1 = wo_voff(l, 1);
        rd(0); rd(1); rd(2); rd(3);
        SB();
        UNROLL for (int s = 0; s < 28; ++s) {
          if (s == 0) { MF32z(qT, wq[0], xf[0][0]); } else { MF32(qT, wq[s % 5], xf[s / 7][s % 7]); }
          if (s + 4 < 28) rd(s + 4);
          if (s & 1) dma16(rWO, s < 14 ? ov0 : ov1, (14 * w + (s >> 1)) * 8 * 896 + 112 * R.b_j, L_WO + (14 * w + (s >> 1)) * 1024);
          if (s == QMID) {
            VMC((QMID + 1) / 2);
            asm volatile("s_barrier" ::: "memory");
            if (w == 1 && l < 16) post(rWS, WS_MB + (l * 16 + R.a_h) * 512 + R.a_p * 8, tag0);
          }
          SB();
        }
        if (pair == 0) {
          a_touch(P, R, l, 4);
          const v4u ones = v4u{0x3f803f80u, 0x3f803f80u, 0x3f803f80u, 0x3f803f80u};
          const unsigned aq = opq(wl + w * 7168);
          f32x16 cq;
          UNROLL for (int i = 0; i < 7; ++i) { const v4u t = tr16(aq + i * 512, aq + i * 512 + 256); if (i == 0) { nMF32z(cq, ones, t); } else { MF32(cq, ones, t); } }
          if (w < 2) gst16<SC1>(lds16(L_WQ + R.a_p * 1792 + w * 1024 + l * 16), rWS, w && l >= 48 ? OOB : l * 16, WS_WPK + R.a_h * 86016 + 57344 + R.a_p * 1792 + w * 1024);
          FENCE1(cq);
          if (h == 0) sts4(L_CEX + (w * 3 + 2) * 128 + l * 4, cq[0]);
          BAR();
          csq = colsum_all(l, 2);
        }
        const v4u a0 = v4u{csq, csq, 0u, 0u};
        nMF32(qT, a0, xa);
        FENCE1(qT);
        f32x16 pq;
        const v4u q0 = pack8(qT, 0), q1 = pack8(qT, 1);
        nMF32z(pq, wp[0], q0); nMF32(pq, wp[1], q1);
        FENCE1(pq);
        float ep[16], en[16], sn;
        dual_exp(pq, h, sc, ep, en, spq, sn);
        const float fn = spq * __builtin_amdgcn_rcpf(sn);
        UNROLL for (int s = 0; s < 2; ++s) {
          phq[s] = v4u{pk2(ep[8 * s], ep[8 * s + 1]), pk2(ep[8 * s + 2], ep[8 * s + 3]), pk2(ep[8 * s + 4], ep[8 * s + 5]), pk2(ep[8 * s + 6], ep[8 * s + 7])};
          phq[2 + s] = v4u{pk2(en[8 * s] * fn, en[8 * s + 1] * fn), pk2(en[8 * s + 2] * fn, en[8 * s + 3] * fn), pk2(en[8 * s + 4] * fn, en[8 * s + 5] * fn), pk2(en[8 * s + 6] * fn, en[8 * s + 7] * fn)};
        }
        asm volatile("" : "+v"(phq[0]), "+v"(phq[1]), "+v"(phq[2]), "+v"(phq[3]), "+v"(spq));
      }
      if (pair == 0) a_issue_all(P, R, l, 0);
      STAMP(4 + 14 * pair);
      if (w == 0 && l < 16) poll(rWS, WS_MB + (R.a_p * 16 + R.a_h) * 512 + l * 8, tag0);
      BAR();
    }
    STAMP(5 + 14 * pair);
    {
      const Role R(c0, w0);
      const unsigned l = opq(lane) & 63, h = l >> 5, w = R.w, par = R.par;
      v4u pt[16];
      UNROLL for (int i = 0; i < 16; ++i) { pt[i] = gld16<SC1>(rWS, l * 16, WS_KVP + pair * MiB + (R.a_h * 16 + i) * 4096 + w * 1024); SB(); }
      {
        v4u sel;
        UNROLL for (int e = 0; e < 4; ++e) {
          const unsigned r0 = ((2 * e) & 3) + 8 * ((2 * e) >> 2) + 4 * h;
          sel[e] = (l & 31) == r0 ? 0x00003f80u : ((l & 31) == r0 + 1 ? 0x3f800000u : 0u);
        }
        f32x16 t;
        nMF32z(t, sel, pt[0]);
        UNROLL for (int i = 1; i < 16; ++i) MF32(t, sel, pt[i]);
        FENCE1(t);
        sts16(L_MISC + w * 1024 + l * 16, pack8(t, 0));
      }
      BAR();
      f32x16 n;
      v4u kf[4];
      const unsigned kb = opq(L_MISC + l * 16);
      UNROLL for (int i = 0; i < 4; ++i) kf[i] = lds16(kb + i * 1024);
      MF32z(n, kf[0], phq[0]); MF32(n, kf[1], phq[1]); MF32(n, kf[2], phq[2]); MF32(n, kf[3], phq[3]);
      FENCE1(n);
      const float ds = par ? n[0] + n[1] : n[12] + n[13];
      auto r = __builtin_amdgcn_permlane32_swap(asu(ds), asu(ds), false, false);
      const float den = asf(par ? r[0] : r[1]);
      const float inv = __builtin_amdgcn_rcpf(den + 1e-6f * spq);
      v2u o[4];
      UNROLL for (int G = 0; G < 4; ++G) o[G] = v2u{pk2(n[4 * G] * inv, n[4 * G + 1] * inv), pk2(n[4 * G + 2] * inv, n[4 * G + 3] * inv)};
      const unsigned Pp = R.a_h >> 1;
      const unsigned so = WS_OF + pair * 2 * MiB + (8 * R.a_p + 2 * w) * 14336 + 7 * (Pp >> 2) * 1024 + (Pp & 3) * 256;
      const unsigned vo = ((l >> 4) & 1) * 14336 + (l & 15) * 16;
      const bool full2 = h == par, useB = par && !h;
      gst16<SC1>(useB ? cat(o[2], o[3]) : cat(o[0], o[1]), rWS, vo + (par ? (h ? 4 : 6) : (h ? 2 : 0)) * 1024, so);
      gst16<SC1>(cat(o[2], o[3]), rWS, full2 ? vo + (par ? 5 : 1) * 1024 : OOB, so);
      gst8<SC1>(par ? o[1] : o[2], rWS, full2 ? OOB : vo + 3 * 1024 + 8 * par, so);
      STAMP(6 + 14 * pair);
      if (pair == 0) a_read<0, 9>(R, l);
      DRAIN();
      BAR();
      if (w == 1 && l < 16) post(rWS, WS_MB + 131072 + ((2 * R.a_p + (l >> 3)) * 8 + (l & 7)) * 512 + R.a_h * 8, tag0 + 1);
      if (pair == 0) a_issue_all(P, R, l, 9);
    }
    {
      const Role R(c0, w0);
      const unsigned l = opq(lane) & 63, g = l >> 4, q4 = (l & 15) >> 2, w = R.w;
      f32x4 acc[4];
      {
        constexpr int PRE = 8, LEAD = 12;
        const unsigned f = ((q4 >> 1) & 1) | ((g & 1) << 1), wb = L_WO + (56 * g + q4) * 128 + (l & 3) * 8;
        unsigned ba[4];
        UNROLL for (int a = 0; a < 4; ++a) ba[a] = opq(wb + 32 * (a ^ f));
        v4u wf[PRE][4];
        auto rd = [&](int s, int a) { wf[s % PRE][a] = tr16(ba[a] + (224 * (s / 7) + rhoO(s % 7, 0)) * 128, ba[a] + (224 * (s / 7) + rhoO(s % 7, 1)) * 128); };
        UNROLL for (int s = 0; s < PRE; ++s) UNROLL for (int a = 0; a < 4; ++a) rd(s, a);
        UNROLL for (int a = 0; a < 4; ++a) acc[a] = f32x4{lo16(res[a].x), hi16(res[a].x), lo16(res[a].y), hi16(res[a].y)};
        asm volatile("" : "+v"(acc[0]), "+v"(acc[1]), "+v"(acc[2]), "+v"(acc[3]));
        if (w == 0 && l < 16) poll(rWS, WS_MB + 131072 + (R.b_q * 8 + R.b_j) * 512 + l * 8, tag0 + 1);
        BAR();
        STAMP(7 + 14 * pair);
        v4u of[14];
        auto ld = [&](int s) { of[s] = gld16<SC1>(rWS, l * 16 + (s & 3) * 1024, WS_OF + pair * 2 * MiB + (4 * R.b_q + w) * 14336 + (s >> 2) * 4096); };
        UNROLL for (int s = 0; s < LEAD; ++s) { ld(s); SB(); }
        UNROLL for (int s = 0; s < 14; ++s)
          UNROLL for (int a = 0; a < 4; ++a) { MF16(acc[a], wf[s % PRE][a], of[s]); if (a == 0 && s + LEAD < 14) ld(s + LEAD); if (s + PRE < 14) rd(s + PRE, a); SB(); }
        FENCE4(acc);
      }
      UNROLL for (int e = 0; e < 4; ++e) acc[3][e] = l >= 32 ? 0.f : acc[3][e];
      float s1 = 0.f, s2 = 0.f;
      UNROLL for (int a = 0; a < 4; ++a) UNROLL for (int e = 0; e < 4; ++e) { s1 += acc[a][e]; s2 = __builtin_fmaf(acc[a][e], acc[a][e], s2); }
      s1 = sum32(sum16(s1)); s2 = sum32(sum16(s2));
      const unsigned tg = (unsigned)tag0 + 2;
      const unsigned g3 = WS_G3 + pair * 262144 + R.b_q * 8192 + w * 256;
      gst16<SC1>(v4u{asu(s1), tg, asu(s2), tg}, rWS, l < 16 ? l * 16 : OOB, g3 + R.b_j * 1024);
      UNROLL for (int a = 0; a < 4; ++a) res[a] = v2u{pk2(acc[a][0], acc[a][1]), pk2(acc[a][2], acc[a][3])};
      STAMP(8 + 14 * pair);
      if (pair == 0) {
        BAR();
        a_read<9, 18>(R, l);
        a_issue_own(P, R, l, 18, 9);
        if (l < 49) touch((w >> 1) ? P.hi : P.hr, (98 * R.rr + 49 * (w & 1) + l) * 128);
      } else __builtin_amdgcn_s_sleep(S3SLEEP);
      v4u ga, gb;
      _Pragma("unroll 1") for (unsigned spin = 0; spin < 0x10000; ++spin) {
        ga = gld16<SC1>(rWS, (l & 15) * 16 + g * 2048, g3); gb = gld16<SC1>(rWS, (l & 15) * 16 + g * 2048 + 1024, g3);
        if (__all(ga.y == tg && ga.w == tg && gb.y == tg && gb.w == tg)) break;
        asm volatile("" ::: "memory");
      }
      STAMP(9 + 14 * pair);
      const float t1 = sum32(sum16(asf(ga.x) + asf(gb.x)));
      const float t2 = sum32(sum16(asf(ga.z) + asf(gb.z)));
      const float mean = t1 * (1.f / 448.f), var = fmaxf(t2 * (1.f / 448.f) - mean * mean, 0.f);
      const float rstd = __builtin_amdgcn_rsqf(var + 1e-5f), nb = -mean * rstd;
      const unsigned img = L_TOP + w * 2048;
      UNROLL for (int a = 0; a < 4; ++a)
        sts8(img + (l & 15) * 128 + 8 * ((4 * a + g) ^ (l & 15)),
             v2u{pk2(__builtin_fmaf(acc[a][0], rstd, nb), __builtin_fmaf(acc[a][1], rstd, nb)), pk2(__builtin_fmaf(acc[a][2], rstd, nb), __builtin_fmaf(acc[a][3], rstd, nb))});
      UNROLL for (int n = 0; n < 2; ++n) {
        const unsigned T = 4 * n + g, o = T & 1, QS = T >> 1, t0 = 8 * o + q4;
        const v4u d = tr16(img + t0 * 128 + 8 * ((4 * QS + (l & 3)) ^ t0), img + (t0 + 4) * 128 + 8 * ((4 * QS + (l & 3)) ^ (t0 + 4)));
        const unsigned quad = 4 * QS + q4, cq = 56 * (R.b_j & 1) + 4 * quad;
        const unsigned tile = cq < 96 ? 2 * (cq >> 5) + ((cq >> 2) & 1) : 6, ii = (cq < 96 ? 4 * ((cq & 31) >> 3) : cq - 96) + (l & 3);
        gst16<SC1>(d, rWS, quad < 14 ? tile * 1024 + ((2 * (w & 1) + o) * 16 + ii) * 16 : OOB,
                   WS_XN + pair * 2 * MiB + (R.b_j >> 1) * 458752 + (2 * R.b_q + (w >> 1)) * 7168);
      }
      if (pair == 0) a_read<18, 27>(R, l);
      DRAIN();
      BAR();
      if (w == 1) post(rWS, WS_MB + 2 * 131072 + (l * 4 + (R.b_j >> 1)) * 512 + (2 * R.b_q + (R.b_j & 1)) * 8, tag0 + 3);
      if (pair == 0) a_issue_own(P, R, l, 27, 5);
      stage_h(P, R, l);
      if (w == 0) poll(rWS, WS_MB + 2 * 131072 + (R.d_t * 4 + R.d_j) * 512 + l * 8, tag0 + 3);
      BAR();
    }
    STAMP(10 + 14 * pair);
    {
      const Role R(c0, w0);
      const unsigned l = opq(lane) & 63, w = R.w;
      constexpr int NB = B1DIST / 7 + 1 + (B1DIST % 7 != 0);
      f32x4 acc[4][7];
      v4u b[NB][7];
      const unsigned xb = WS_XN + pair * 2 * MiB + R.d_j * 458752 + 16 * w * 7168;
      auto ldb = [&](int i) { const int s = i / 7, n = i % 7; b[s % NB][n] = gld16<SC1>(rWS, l * 16 + (n & 3) * 1024, xb + ((s + R.rot) & 15) * 7168 + (n >> 2) * 4096); };
      UNROLL for (int i = 0; i < B1DIST; ++i) { ldb(i); SB(); }
      UNROLL for (int s = 0; s < 16; ++s) {
        UNROLL for (int n = 0; n < 7; ++n) {
          UNROLL for (int m = 0; m < 4; ++m) {
            if (s == 0) { MF16Az(acc[m][n], b[0][n], m); } else { MF16A(acc[m][n], b[s % NB][n], 4 * s + m); }
            if (m == 0 && 7 * s + n + B1DIST < 112) ldb(7 * s + n + B1DIST);
          }
          SB();
        }
        if (s == 1 && pair == 0) a_read<27, 32>(R, l);
      }
      asm volatile("s_nop 7");
      PIN7(acc[0]); PIN7(acc[1]); PIN7(acc[2]); PIN7(acc[3]);
      STAMP(11 + 14 * pair);
      const unsigned r1 = opq(L_T + w * 14336 + l * 16), r2 = opq(L_T + (w ^ 1) * 14336 + l * 16), r3 = opq(L_T + (w ^ 3) * 14336 + l * 16);
      UNROLL for (int n = 0; n < 7; ++n) sts16(r1 + (7 + n) * 1024, __builtin_bit_cast(v4u, acc[3][n]));
      UNROLL for (int n = 0; n < 7; ++n) sts16(r1 + n * 1024, __builtin_bit_cast(v4u, acc[1][n]));
      BAR();
      UNROLL for (int n = 0; n < 7; ++n) acc[2][n] += __builtin_bit_cast(f32x4, lds16(r2 + (7 + n) * 1024));
      UNROLL for (int n = 0; n < 7; ++n) sts16(r2 + (7 + n) * 1024, __builtin_bit_cast(v4u, acc[2][n]));
      acc[0][6] += __builtin_bit_cast(f32x4, lds16(r2 + 6 * 1024));
      sts16(r2 + 6 * 1024, __builtin_bit_cast(v4u, acc[0][6]));
      UNROLL for (int n = 0; n < 6; ++n) acc[0][n] += __builtin_bit_cast(f32x4, lds16(r2 + n * 1024));
      BAR();
      UNROLL for (int n = 0; n < 7; ++n) acc[0][n] += __builtin_bit_cast(f32x4, lds16(r3 + (7 + n) * 1024));
      const f32x4 other = acc[2][6] + __builtin_bit_cast(f32x4, lds16(r3 + 6 * 1024));
      const unsigned sg = (w >> 1) ? 0x80008000u : 0u;
      v2u o[7];
      UNROLL for (int n = 0; n < 7; ++n) o[n] = v2u{pk2(acc[0][n][0], acc[0][n][1]) ^ sg, pk2(acc[0][n][2], acc[0][n][3]) ^ sg};
      const unsigned so = WS_PQ + pair * 4 * MiB + (2 * R.d_t + (w & 1)) * 28672 + 7 * R.d_j * 1024;
      UNROLL for (int u = 0; u < 3; ++u) gst16<SC1>(cat(o[2 * u], o[2 * u + 1]), rWS, l * 16 + u * 1024, so + (w >> 1) * 3072);
      if (w < 2) gst16<SC1>(cat(o[6], v2u{pk2(other[0], other[1]) ^ 0x80008000u, pk2(other[2], other[3]) ^ 0x80008000u}), rWS, l * 16, so + 6144);
      STAMP(12 + 14 * pair);
      DRAIN();
      BAR();
      if (w == 1 && l < 8) post(rWS, WS_MB + 3 * 131072 + ((R.d_t >> 1) * 8 + l) * 512 + (4 * (R.d_t & 1) + R.d_j) * 8, tag0 + 4);
      if (pair == 0) {
        if (PRETOUCH && w >= 2 && l < 36) ws_touch(P, R, 36 * (w - 2) + l, 1);
        FOR_3WAVES(56, n) dma16c(rWS, l * 16, WS_WPK + R.a_h * 86016 + n * 1024, L_T + n * 1024);
      }
    }
    {
      const Role R(c0, w0);
      const unsigned l = opq(lane) & 63, g = l >> 4, e16 = l & 15, f = (e16 >> 1) & 7, w = R.w;
      f32x4 acc[4];
      {
        constexpr int PRE = 7, LEAD = 12;
        unsigned hb[2][4], hm[2][4];
        UNROLL for (int k = 0; k < 4; ++k) {
          hb[0][k] = opq(((2 * k + g) >> 3) * 7168 + e16 * 128 + 16 * (((2 * k + g) & 7) ^ f)); hb[1][k] = opq(hb[0][k] + L_HI);
          hm[0][k] = opq(e16 * 128 + 16 * ((2 * k + (g >> 1)) ^ f) + 8 * (g & 1)); hm[1][k] = opq(hm[0][k] + L_HI);
        }
        v4u wf[PRE][4];
        auto rd = [&](int s, int a) {
          const int j = s / 7, i = s % 7, c = 14 * j + (i < 6 ? 4 * (i % 3) : 12);
          if (i < 6) wf[s % PRE][a] = lds16(hb[i / 3][(c & 7) >> 1] + (c >> 3) * 7168 + a * 2048);
          else wf[s % PRE][a] = cat(lds8(hm[0][(c & 7) >> 1] + (c >> 3) * 7168 + a * 2048), lds8(hm[1][(c & 7) >> 1] + (c >> 3) * 7168 + a * 2048));
        };
        UNROLL for (int s = 0; s < PRE; ++s) { UNROLL for (int a = 0; a < 4; ++a) { rd(s, a); asm volatile("" ::: "memory"); } }
        UNROLL for (int a = 0; a < 4; ++a) acc[a] = f32x4{lo16(res[a].x), hi16(res[a].x), lo16(res[a].y), hi16(res[a].y)};
        asm volatile("" : "+v"(acc[0]), "+v"(acc[1]), "+v"(acc[2]), "+v"(acc[3]));
        if (w == 0 && l < 8) poll(rWS, WS_MB + 3 * 131072 + (R.b_q * 8 + R.b_j) * 512 + l * 8, tag0 + 4);
        BAR();
        STAMP(13 + 14 * pair);
        v4u pf[28];
        auto ld = [&](int s) { pf[s] = gld16<SC1>(rWS, l * 16 + (s & 3) * 1024, WS_PQ + pair * 4 * MiB + (4 * R.b_q + w) * 28672 + (s >> 2) * 4096); };
        UNROLL for (int s = 0; s < LEAD; ++s) { ld(s); SB(); }
        UNROLL for (int s = 0; s < 28; ++s)
          UNROLL for (int a = 0; a < 4; ++a) { MF16(acc[a], wf[s % PRE][a], pf[s]); if (a == 0 && s + LEAD < 28) ld(s + LEAD); if (s + PRE < 28) rd(s + PRE, a); SB(); }
        FENCE4(acc);
      }
      UNROLL for (int a = 0; a < 4; ++a) res[a] = v2u{pk2(acc[a][0], acc[a][1]), pk2(acc[a][2], acc[a][3])};
      if (pair == 1) {
        const rsrc_t rO = mk(P.out, 2048 * 448 * 2);
        const unsigned ro = (64 * R.b_q + 16 * w + e16) * 896 + (56 * R.b_j + 4 * g) * 2;
        UNROLL for (int a = 0; a < 4; ++a) gst8<0>(res[a], rO, (a == 3 && l >= 32) ? OOB : ro + 32 * a, 0);
        UNROLL for (int Js = 0; Js < 4; ++Js)
          asm volatile("" : "=v"(xf[Js][0]), "=v"(xf[Js][1]), "=v"(xf[Js][2]), "=v"(xf[Js][3]), "=v"(xf[Js][4]), "=v"(xf[Js][5]), "=v"(xf[Js][6]));
        asm volatile("" : "=v"(kT), "=v"(vv), "=v"(ssq));
      } else {
        const unsigned so = WS_X2 + (2 * R.b_q + (w >> 1)) * 28672 + 7 * (R.b_j >> 1) * 1024 + (R.b_j & 1) * 512 + (w & 1) * 256;
        gst16<SC1>(cat(res[0], res[1]), rWS, g * 1024 + e16 * 16, so);
        gst16<SC1>(cat(res[2], res[3]), rWS, l < 32 ? (4 + g) * 1024 + e16 * 16 : OOB, so);
        gst8<SC1>(res[2], rWS, l < 32 ? OOB : 6 * 1024 + e16 * 16 + (g & 1) * 8, so);
        float s2 = 0.f, s3 = 0.f;
        UNROLL for (int a = 0; a < 3; ++a) UNROLL for (int e = 0; e < 4; ++e) s2 = __builtin_fmaf(acc[a][e], acc[a][e], s2);
        UNROLL for (int e = 0; e < 4; ++e) s3 = __builtin_fmaf(acc[3][e], acc[3][e], s3);
        s2 = sum32(sum16(s2 + (l < 32 ? s3 : 0.f)));
        __builtin_amdgcn_raw_buffer_store_b32(asu(s2), rWS, l < 32 ? l * 4 : OOB, WS_SSQ + (4 * R.b_q + w) * 1024 + R.b_j * 128, SC1);
        STAMP(14);
        DRAIN();
        BAR();
        if (w == 1 && l < 16) post(rWS, WS_MB + 4 * 131072 + ((R.b_q >> 1) * 16 + l) * 512 + (8 * (R.b_q & 1) + R.b_j) * 8, tag0 + 5);
        FOR_3WAVES(28, n) dma16c(rWS, l * 16, WS_WPK + R.a_h * 86016 + 57344 + n * 1024, L_WQ + n * 1024);
        if (w == 0 && l < 16) poll(rWS, WS_MB + 4 * 131072 + (R.a_p * 16 + R.a_h) * 512 + l * 8, tag0 + 5);
        BAR();
        STAMP(15);
        constexpr int LEAD = 12;
        auto ld = [&](int s) { xf[s / 7][s % 7] = gld16<SC1>(rWS, l * 16 + ((s % 7) & 3) * 1024, WS_X2 + (4 * R.a_p + w) * 28672 + ((s / 7) ^ R.j0) * 7168 + ((s % 7) >> 2) * 4096); };
        UNROLL for (int s = 0; s < LEAD; ++s) { ld(s); SB(); }
        unsigned sq[4];
        kv_gemm(l, R.j0, [&](int s, const v4u&, const v4u&) {
          if (s + LEAD < 28) ld(s + LEAD);
          else if (s + LEAD < 32) sq[s + LEAD - 28] = __builtin_amdgcn_raw_buffer_load_b32(rWS, ((l >> 4) & 1) * 1024 + (l >> 5) * 512 + (l & 15) * 4 + (s + LEAD - 28) * 128, WS_SSQ + (8 * R.a_p + 2 * w) * 1024, SC1);
        });
        ssq = (asf(sq[0]) + asf(sq[1])) + (asf(sq[2]) + asf(sq[3]));
        FENCE2(kT, vv);
      }
    }
  }
  if (c0 == 0 && w0 == 0 && lane == 0) {
    const u64 nn = nonce + 16;
    gst8<SC1>(v2u{(unsigned)nn, (unsigned)(nn >> 32)}, rWS, 0, WS_NONCE);
  }
  asm volatile("Lfused_end:");
}

}

extern "C" void kernel_launch(void* const* d_in, const int* in_sizes, int n_in, void* d_out, int out_size, void* d_ws, size_t ws_size,
                              hipStream_t stream) {
  fused::Params p;
  p.x = d_in[0]; p.wqkv = d_in[1]; p.wphi = d_in[2]; p.wo = d_in[3];
  p.cr = d_in[5]; p.ci = d_in[6]; p.hr = d_in[7]; p.hi = d_in[8];
  p.out = d_out; p.ws = d_ws;
  fused::fused_fwd<<<256, 256, 0, stream>>>(p);
}
```

```cpp
#include <hip/hip_runtime.h>
#include <hip/hip_bf16.h>
#include <stdint.h>

namespace fused {

typedef unsigned v4u __attribute__((ext_vector_type(4)));
typedef unsigned v2u __attribute__((ext_vector_type(2)));
typedef float f32x2 __attribute__((ext_vector_type(2)));
typedef float f32x4 __attribute__((ext_vector_type(4)));
typedef float f32x16 __attribute__((ext_vector_type(16)));
typedef __bf16 bf16x2 __attribute__((ext_vector_type(2)));
typedef __bf16 bf16x8 __attribute__((ext_vector_type(8)));
typedef short s16x4 __attribute__((ext_vector_type(4)));
typedef __amdgpu_buffer_rsrc_t rsrc_t;
typedef unsigned long long u64;
#define LAS __attribute__((address_space(3)))
#define DI __device__ __forceinline__

constexpr unsigned MiB = 1u << 20;
constexpr unsigned WS_NONCE = 0;
constexpr unsigned WS_MB = 4096;
constexpr unsigned WS_G3 = 1 * MiB;
constexpr unsigned WS_KVP = 2 * MiB;
constexpr unsigned WS_OF = 4 * MiB;
constexpr unsigned WS_XN = 8 * MiB;
constexpr unsigned WS_PQ = 12 * MiB;
constexpr unsigned WS_X2 = 20 * MiB;
constexpr unsigned WS_SSQ = 22 * MiB;
constexpr unsigned WS_WPK = 22 * MiB + 262144;
constexpr unsigned WS_STAMP = 23 * MiB + 786432;
constexpr unsigned WS_BYTES = 24 * MiB;
constexpr unsigned OOB = 0x7FFFFFF0u;
constexpr int SC1 = 16;

constexpr unsigned L_WQ = 0, L_WO = 28672, L_MISC = 86016, L_SMALL = 102400, L_T = 106496, L_TOP = 155648;
constexpr unsigned L_WPHI = L_SMALL, L_CEX = L_SMALL + 1664, L_STAMP = L_SMALL + 3328, L_JUNK = L_SMALL + 3840;
constexpr unsigned L_HI = 50176;
constexpr unsigned LDS_BYTES = 163840;

struct Params {
  const void *x, *wqkv, *wphi, *wo, *cr, *ci, *hr, *hi;
  void *out, *ws;
};

#define MF16(c, a, b) asm volatile("v_mfma_f32_16x16x32_bf16 %0, %1, %2, %0" : "+v"(c) : "v"(a), "v"(b))
#define MF16z(c, a, b) asm volatile("v_mfma_f32_16x16x32_bf16 %0, %1, %2, 0" : "=&v"(c) : "v"(a), "v"(b))
#define nMF16(c, a, b) asm volatile("s_nop 1\n\tv_mfma_f32_16x16x32_bf16 %0, %1, %2, %0" : "+v"(c) : "v"(a), "v"(b))
#define MF32(c, a, b) asm volatile("v_mfma_f32_32x32x16_bf16 %0, %1, %2, %0" : "+v"(c) : "v"(a), "v"(b))
#define MF32z(c, a, b) asm volatile("v_mfma_f32_32x32x16_bf16 %0, %1, %2, 0" : "=&v"(c) : "v"(a), "v"(b))
#define nMF32(c, a, b) asm volatile("s_nop 1\n\tv_mfma_f32_32x32x16_bf16 %0, %1, %2, %0" : "+v"(c) : "v"(a), "v"(b))
#define nMF32z(c, a, b) asm volatile("s_nop 1\n\tv_mfma_f32_32x32x16_bf16 %0, %1, %2, 0" : "=&v"(c) : "v"(a), "v"(b))
#define MF16A(c, a, R) asm volatile("v_mfma_f32_16x16x32_bf16 %0, %1, a[%c2:%c3], %0" : "+v"(c) : "v"(a), "i"(4 * (R)), "i"(4 * (R) + 3))
#define MF16Az(c, a, R) asm volatile("v_mfma_f32_16x16x32_bf16 %0, %1, a[%c2:%c3], 0" : "=&v"(c) : "v"(a), "i"(4 * (R)), "i"(4 * (R) + 3))
#define FENCE1(c) asm volatile("s_nop 11" : "+v"(c))
#define FENCE2(c, d) asm volatile("s_nop 11" : "+v"(c), "+v"(d))
#define FENCE4(c) asm volatile("s_nop 7" : "+v"(c[0]), "+v"(c[1]), "+v"(c[2]), "+v"(c[3]))
#define PIN7(c) asm volatile("" : "+v"(c[0]), "+v"(c[1]), "+v"(c[2]), "+v"(c[3]), "+v"(c[4]), "+v"(c[5]), "+v"(c[6]))
#define A_LDS_RD(R, addr, off) asm volatile("ds_read_b128 a[%c0:%c1], %2 offset:%c3" ::"i"(4 * (R)), "i"(4 * (R) + 3), "v"(addr), "i"(off) : "memory")
DI float asf(unsigned u) { return __builtin_bit_cast(float, u); }
DI unsigned asu(float f) { return __builtin_bit_cast(unsigned, f); }
DI unsigned pk2(float a, float b) { return __builtin_bit_cast(unsigned, __builtin_convertvector(f32x2{a, b}, bf16x2)); }
DI float lo16(unsigned u) { return asf(u << 16); }
DI float hi16(unsigned u) { return asf(u & 0xffff0000u); }
DI float dot2(unsigned a, unsigned b, float c) { return __builtin_amdgcn_fdot2_f32_bf16(__builtin_bit_cast(bf16x2, a), __builtin_bit_cast(bf16x2, b), c, false); }
DI v4u lds16(unsigned a) { return *(LAS const v4u*)(size_t)a; }
DI v2u lds8(unsigned a) { return *(LAS const v2u*)(size_t)a; }
DI float lds4(unsigned a) { return *(LAS const float*)(size_t)a; }
DI void sts16(unsigned a, v4u v) { *(LAS v4u*)(size_t)a = v; }
DI void sts8(unsigned a, v2u v) { *(LAS v2u*)(size_t)a = v; }
DI void sts4(unsigned a, float v) { *(LAS float*)(size_t)a = v; }
DI v2u tr8(unsigned a) { return __builtin_bit_cast(v2u, __builtin_amdgcn_ds_read_tr16_b64_v4i16((LAS s16x4*)(size_t)a)); }
DI v4u cat(v2u a, v2u b) { return v4u{a.x, a.y, b.x, b.y}; }
DI v4u tr16(unsigned a0, unsigned a1) { return cat(tr8(a0), tr8(a1)); }
template <int AUX> DI v4u gld16(rsrc_t r, unsigned v, unsigned s) { return __builtin_amdgcn_raw_buffer_load_b128(r, v, s, AUX); }
template <int AUX> DI v2u gld8(rsrc_t r, unsigned v, unsigned s) { return __builtin_amdgcn_raw_buffer_load_b64(r, v, s, AUX); }
template <int AUX> DI void gst16(v4u d, rsrc_t r, unsigned v, unsigned s) { __builtin_amdgcn_raw_buffer_store_b128(d, r, v, s, AUX); }
template <int AUX> DI void gst8(v2u d, rsrc_t r, unsigned v, unsigned s) { __builtin_amdgcn_raw_buffer_store_b64(d, r, v, s, AUX); }
DI rsrc_t mk(const void* p, unsigned bytes) { return __builtin_amdgcn_make_buffer_rsrc((void*)p, 0, bytes, 0x00020000); }
DI void dma16c(rsrc_t r, unsigned v, unsigned s, unsigned lds) {
  asm volatile("s_mov_b32 m0, %3\n\ts_nop 0\n\tbuffer_load_dwordx4 %0, %1, %2 offen sc1 lds" ::"v"(v), "s"(r), "s"(s), "s"(lds) : "memory");
}
DI void dma16(rsrc_t r, unsigned v, unsigned s, unsigned lds) {
  asm volatile("s_mov_b32 m0, %3\n\ts_nop 0\n\tbuffer_load_dwordx4 %0, %1, %2 offen lds" ::"v"(v), "s"(r), "s"(s), "s"(lds) : "memory");
}
DI void touch(const void* base, unsigned off) {
  asm volatile("s_mov_b32 m0, %1\n\ts_nop 0\n\tglobal_load_lds_dword %0, off" ::"v"((const char*)base + off), "s"(L_JUNK) : "memory");
}
#define VMC(n) asm volatile("s_waitcnt vmcnt(%c0)" ::"i"(n) : "memory")
#define BAR() asm volatile("s_waitcnt lgkmcnt(0)\n\ts_barrier" ::: "memory")
#define DRAIN() asm volatile("s_waitcnt vmcnt(0)" ::: "memory")
#define LGKM0() asm volatile("s_waitcnt lgkmcnt(0)" ::: "memory")
#define SB() __builtin_amdgcn_sched_barrier(0)
#define UNROLL _Pragma("unroll")
DI unsigned opq(unsigned v) { asm volatile("" : "+v"(v)); return v; }
DI unsigned sopq(unsigned v) { asm volatile("" : "+s"(v)); return v; }
DI float sum32(float v) { auto r = __builtin_amdgcn_permlane32_swap(asu(v), asu(v), false, false); return asf(r[0]) + asf(r[1]); }
DI float sum16(float v) { auto r = __builtin_amdgcn_permlane16_swap(asu(v), asu(v), false, false); return asf(r[0]) + asf(r[1]); }
DI float max32(float v) { auto r = __builtin_amdgcn_permlane32_swap(asu(v), asu(v), false, false); return fmaxf(asf(r[0]), asf(r[1])); }
DI float min32(float v) { auto r = __builtin_amdgcn_permlane32_swap(asu(v), asu(v), false, false); return fminf(asf(r[0]), asf(r[1])); }

DI void post(rsrc_t ws, unsigned voff, u64 tag) {
  asm volatile("buffer_atomic_swap_x2 %0, %1, %2, 0 offen\n\ts_nop 1" ::"v"(tag), "v"(voff), "s"(ws) : "memory");
}
DI void poll(rsrc_t ws, unsigned voff, u64 tag) {
  unsigned cnt = 0; u64 t;
  asm volatile(
      "Lp%=:\n\tv_mov_b64 %1, 0\n\ts_nop 1\n\tbuffer_atomic_add_x2 %1, %2, %3, 0 offen sc0\n\ts_add_u32 %0, %0, 1\n\ts_waitcnt vmcnt(0)\n\t"
      "v_cmp_eq_u64 vcc, %4, %1\n\ts_nop 1\n\ts_cmp_eq_u64 vcc, exec\n\ts_cbranch_scc1 Ld%=\n\t"
      "s_cmp_lt_u32 %0, 0x10000\n\ts_cbranch_scc1 Lp%=\n\tLd%=:"
      : "+s"(cnt), "=&v"(t) : "v"(voff), "s"(ws), "s"(tag) : "vcc", "scc", "memory");
}

#define RT(t) asm volatile("s_memrealtime %0\n\ts_waitcnt lgkmcnt(0)" : "=s"(t)::"memory")
#define STAMP(i) do { if ((i) == 0) RT(pt0_); if ((i) == 0) RT(ptk_); } while (0)
#define CSTAMP(i) do {} while (0)

DI constexpr int rhoA(int i, int t) { return i < 4 ? (t ? 16 + 4 * i : 4 * i) : (i < 6 ? (t ? 48 + 4 * (i - 4) : 32 + 4 * (i - 4)) : (t ? 44 : 40)); }
DI constexpr int rhoO(int i, int t) { return i == 0 ? (t ? 8 : 0) : i == 1 ? (t ? 24 : 16) : i == 2 ? (t ? 12 : 4) : i == 3 ? (t ? 32 : 20) : i == 4 ? (t ? 36 : 28) : i == 5 ? (t ? 52 : 44) : (t ? 48 : 40); }

DI v4u pack8(const f32x16& x, int s) { return v4u{pk2(x[8 * s], x[8 * s + 1]), pk2(x[8 * s + 2], x[8 * s + 3]), pk2(x[8 * s + 4], x[8 * s + 5]), pk2(x[8 * s + 6], x[8 * s + 7])}; }

DI void dual_exp(const f32x16& p, bool h, float sc, float (&ep)[16], float (&en)[16], float& sp, float& sn) {
  float q[16];
  UNROLL for (int i = 0; i < 16; ++i) q[i] = (i >= 12 && h) ? p[0] : p[i];
  float mx = q[0], mn = q[0];
  UNROLL for (int i = 1; i < 16; ++i) { mx = fmaxf(mx, q[i]); mn = fminf(mn, q[i]); }
  mx = max32(mx); mn = min32(mn);
  const float a = -mx * sc, b = mn * sc;
  sp = 0.f; sn = 0.f;
  UNROLL for (int i = 0; i < 16; ++i) {
    float e = __builtin_amdgcn_exp2f(__builtin_fmaf(q[i], sc, a)), f = __builtin_amdgcn_exp2f(__builtin_fmaf(q[i], -sc, b));
    if (i >= 12) { e = h ? 0.f : e; f = h ? 0.f : f; }
    ep[i] = e; en[i] = f; sp += e; sn += f;
  }
  sp = sum32(sp); sn = sum32(sn);
}

#define S3SLEEP 4
#define QMID 17
#define B1GR 8
#define B1DIST 14
#define PRETOUCH 1
struct Role {
  unsigned w, xcd, rr, a_p, a_h, par, j0, b_q, b_j, d_t, d_j, rot;
  DI Role(unsigned c0, unsigned w0) {
    const unsigned c = sopq(c0);
    xcd = c & 7; rr = c >> 3; w = sopq(w0);
    a_p = 4 * (xcd >> 1) + (rr >> 3); a_h = 8 * (xcd & 1) + (rr & 7); par = a_h & 1;
    j0 = (w ^ a_h) & 3;
    b_q = 4 * xcd + (rr >> 3); b_j = rr & 7;
    d_t = 8 * xcd + (rr >> 2); d_j = rr & 3;
    rot = B1GR * (d_t & 1);
  }
};

DI unsigned w_voff(unsigned l) { return (l >> 2) * 2688 + (l & 3) * 16; }
DI void stage_w1(const Params& P, const Role& R, unsigned v, unsigned part, unsigned n, unsigned dst) {
  dma16(mk(P.wqkv, 448 * 1344 * 2), v, n * (16 * 2688) + part * 896 + ((56 * R.a_h) & ~15u), dst + n * 1024);
}
#define FOR_3WAVES(N, n) _Pragma("unroll 1") for (unsigned n = R.w ? R.w - 1 : (N); n < (N); n += 3)
DI void patch_pad(const Role& R, unsigned l) {
  const unsigned a = L_T + (112 * R.w + l) * 64 + (R.par ? 0 : 56);
  sts8(a, v2u{0x00003f80u, 0u}); sts8(a + 28672, v2u{0u, 0u});
  if (l < 48) { sts8(a + 4096, v2u{0x00003f80u, 0u}); sts8(a + 4096 + 28672, v2u{0u, 0u}); }
}
DI unsigned wo_voff(unsigned l, int hf) { return (l >> 3) * 896 + 16 * (2 * (((l & 7) >> 1) ^ (((l >> 4) & 1) | (hf << 1))) + (l & 1)); }
DI void stage_h(const Params& P, const Role& R, unsigned l) {
  const unsigned v0 = (l >> 3) * 896 + 16 * ((l & 7) ^ (l >> 4)), v1 = (l >> 3) * 896 + 16 * ((l & 7) ^ (4 + (l >> 4)));
  FOR_3WAVES(14, n) {
    const unsigned m = n >= 7, u = n - 7 * m;
    const rsrc_t rH = mk(m ? P.hi : P.hr, 448 * 448 * 2);
    UNROLL for (int o = 0; o < 7; ++o) dma16(rH, (o & 1) ? v1 : v0, (56 * R.b_j + 8 * o) * 896 + u * 128, n * 7168 + o * 1024);
  }
}
DI unsigned a_voff(unsigned l, int o) { return (8 * o + (l >> 3)) * 4096 + 16 * ((l & 7) ^ ((4 * o + (l >> 4)) & 7)); }
DI void a_piece(const Params& P, const Role& R, unsigned v0, unsigned v1, unsigned w, unsigned n, unsigned j) {
  const rsrc_t r = mk(((n ^ w) & 2) ? P.ci : P.cr, 2048 * 2048 * 2);
  const unsigned so = (32 * R.d_t + 16 * ((n ^ w) & 1)) * 4096 + 1024 * w + 128 * (((n >> 2) + (R.rot >> 1)) & 7);
  const unsigned dst = j < 6 ? L_T + w * 12288 + j * 2048 : L_WQ + w * 6144 + (j - 6) * 2048;
  dma16(r, v0, so, dst); dma16(r, v1, so, dst + 1024);
}
DI void a_issue_all(const Params& P, const Role& R, unsigned l, unsigned n0) {
  const unsigned v0 = a_voff(l, 0), v1 = a_voff(l, 1);
  FOR_3WAVES(36, i) a_piece(P, R, v0, v1, i & 3, n0 + (i >> 2), i >> 2);
}
DI void a_issue_own(const Params& P, const Role& R, unsigned l, unsigned n0, unsigned cnt) {
  const unsigned v0 = a_voff(l, 0), v1 = a_voff(l, 1);
  _Pragma("unroll 1") for (unsigned j = 0; j < cnt; ++j) a_piece(P, R, v0, v1, R.w, n0 + j, j);
}
template <int N0, int N1> DI void a_read(const Role& R, unsigned l) {
  const unsigned t = l & 15, f = (t >> 1) & 7, b0 = t * 128 + 16 * ((l >> 4) ^ f), b1 = t * 128 + 16 * ((4 + (l >> 4)) ^ f);
  const unsigned x0 = b0 + L_T + R.w * 12288, x1 = b1 + L_T + R.w * 12288, y0 = b0 + L_WQ + R.w * 6144, y1 = b1 + L_WQ + R.w * 6144;
  UNROLL for (int n = N0; n < N1; ++n) {
    const int j = n - N0, r = 8 * (n >> 2) + (n & 3);
    if (j < 6) { A_LDS_RD(r, x0, j * 2048); A_LDS_RD(r + 4, x1, j * 2048); }
    else { A_LDS_RD(r, y0, (j - 6) * 2048); A_LDS_RD(r + 4, y1, (j - 6) * 2048); }
  }
  LGKM0();
}
DI void a_touch(const Params& P, const Role& R, unsigned l, unsigned u0) {
  touch((R.d_j >> 1) ? P.ci : P.cr, (32 * R.d_t + 16 * (R.d_j & 1) + (l >> 2)) * 4096 + 1024 * R.w + 128 * ((u0 + (l & 3) + (R.rot >> 1)) & 7));
}
DI void ws_touch(const Params& P, const Role& R, unsigned i, unsigned pair) {
  const unsigned Pp = R.a_h >> 1;
  const unsigned o1 = WS_XN + pair * 2 * MiB + (R.b_j >> 1) * 458752 + (2 * R.b_q + (R.b_j & 1)) * 7168 + i * 128;
  const unsigned o2 = WS_OF + pair * 2 * MiB + (8 * R.a_p + (i & 7)) * 14336 + (7 * (Pp >> 2) + 3) * 1024 + (Pp & 3) * 256 + R.par * 128;
  const unsigned o3 = WS_G3 + pair * 262144 + R.b_q * 8192 + R.b_j * 1024 + (i & 7) * 128;
  touch(P.ws, i < 56 ? o1 : (i < 64 ? o2 : o3));
}
DI unsigned colsum_all(unsigned l, int part) {
  const unsigned cx = opq(L_CEX + (l & 31) * 4);
  float s = 0.f;
  UNROLL for (int i = 0; i < 4; ++i) s += lds4(cx + (i * 3 + part) * 128);
  return pk2(s, s - lo16(pk2(s, s)));
}

__global__ void __launch_bounds__(256, 1) fused_fwd(Params P) {
  __shared__ __attribute__((aligned(1024))) char smem[LDS_BYTES];
  asm volatile("" ::"v"((unsigned)(size_t)(LAS char*)smem) : "memory", "a255");
  const char* code; unsigned code_len;
  asm volatile("s_getpc_b64 %0\nLfused_begin:\n\ts_mov_b32 %1, Lfused_end-Lfused_begin" : "=s"(code), "=s"(code_len));
  const unsigned lane = threadIdx.x & 63;
  const unsigned w0 = __builtin_amdgcn_readfirstlane(threadIdx.x >> 6), c0 = blockIdx.x;
  const rsrc_t rWS = mk(P.ws, WS_BYTES);
  u64 pt0_ = 0, ptk_ = 0;
  STAMP(0);

  v2u res[4];
  v4u xf[4][7];
  f32x16 kT, vv;
  float ssq;
  unsigned csk, csv, csq = 0;
  const u64 nonce = *(const __attribute__((address_space(4))) u64*)P.ws;
  auto kv_gemm = [&](unsigned l, unsigned j0, auto&& hook) {
    const unsigned wl = L_T + (56 * (l >> 5) + ((l & 15) >> 2)) * 64 + ((l >> 4) & 1) * 32 + (l & 3) * 8;
    v4u wk[3], wv[3];
    unsigned a;
    auto rd = [&](int s) {
      if (s % 7 == 0) a = opq(wl + ((s / 7) ^ j0) * 7168);
      wk[s % 3] = tr16(a + rhoA(s % 7, 0) * 64, a + rhoA(s % 7, 1) * 64); wv[s % 3] = tr16(a + 28672 + rhoA(s % 7, 0) * 64, a + 28672 + rhoA(s % 7, 1) * 64);
    };
    rd(0); rd(1);
    SB();
    UNROLL for (int s = 0; s < 28; ++s) {
      const v4u x = xf[s / 7][s % 7], k = wk[s % 3], b = wv[s % 3];
      if (s == 0) { MF32z(kT, k, x); } else { MF32(kT, k, x); }
      if (s + 2 < 28) rd(s + 2);
      SB();
      if (s == 0) { MF32z(vv, x, b); } else { MF32(vv, x, b); }
      hook(s, k, b);
      SB();
    }
  };

  {
    const Role R(c0, w0);
    const unsigned l = opq(lane) & 63, w = R.w, j0 = R.j0;
    {
      const unsigned i = 64 * w + l, j = i - 112, r12 = (j * 5462) >> 16, k12 = j - 12 * r12;
      const unsigned ox = (R.xcd >> 1) * (512 * 896) + (112 * R.rr + i) * 128;
      const unsigned ow = (14 * R.rr + r12) * 2688 + (k12 >> 2) * 896 + ((R.xcd & 1) * 3 + (k12 & 3)) * 128;
      touch(i < 112 ? P.x : P.wqkv, i < 112 ? ox : ow);
      const unsigned i2 = 16 * w + l, j2 = i2 + 144, r2 = (j2 * 5462) >> 16, k2 = j2 - 12 * r2;
      const unsigned ow2 = (14 * R.rr + r2) * 2688 + (k2 >> 2) * 896 + ((R.xcd & 1) * 3 + (k2 & 3)) * 128;
      const unsigned op = (R.a_h * 1568 + (i2 - 24) * 128) & ~3u, oc = (R.rr * 12 + i2 - 37) * 128;
      if (l < 16 && i2 < 49) touch(i2 < 24 ? P.wqkv : (i2 < 37 ? P.wphi : code), i2 < 24 ? ow2 : (i2 < 37 ? op : (oc < code_len ? oc : 0)));
      if (l < 2) {
        const unsigned k = 2 * w + l, kind = k < 2 ? k : (k < 6 ? 2 : k - 3);
        const unsigned id = (kind == 0 || kind == 4) ? R.a_p * 16 + R.a_h : (kind == 2 ? R.d_t * 4 + R.d_j : R.b_q * 8 + R.b_j);
        asm volatile("buffer_atomic_add %0, %1, %2, 0 offen" ::"v"(0u), "v"(WS_MB + kind * 131072 + id * 512 + (kind == 2 ? (k - 2) * 128 : 0)), "s"(rWS) : "memory");
      }
    }
    const rsrc_t rX = mk(P.x, 2048 * 448 * 2);
    const unsigned wv0 = w_voff(l);
    _Pragma("unroll 1") for (unsigned n = 7 * w; n < 7 * w + 7; ++n) { stage_w1(P, R, wv0, 1, n, L_T); stage_w1(P, R, wv0, 2, n, L_T + 28672); }
    const unsigned win = w * 16384, xs = (128 * R.a_p + 32 * w) * 896, xc = 16 * ((l & 31) ^ (l >> 5)), xr = (l >> 5) * 896;
    auto x_issue = [&](unsigned hf) { _Pragma("unroll 2") for (unsigned n = 0; n < 16; ++n) dma16(rX, (xc ^ ((32 * n) & 0xf0)) + xr, xs + 384 * hf + n * 1792, win + n * 1024); };
    x_issue(j0 >> 1);
    const unsigned xb = opq(win + (l & 31) * 512), sw = (l & 15) << 4;
    auto x_read = [&](int Js, int i) {
      const unsigned J = j0 ^ Js, o = 224 * J - 384 * (J >> 1) + 112 * (l >> 5);
      xf[Js][i] = cat(lds8(xb + ((o + 2 * rhoA(i, 0)) ^ sw)), lds8(xb + ((o + 2 * rhoA(i, 1)) ^ sw)));
    };
    float q0 = 0.f, q1 = 0.f, q2 = 0.f, q3 = 0.f;
    auto sq = [&](int Js) { UNROLL for (int i = 0; i < 7; ++i) { const v4u x = xf[Js][i]; q0 = dot2(x.x, x.x, q0); q1 = dot2(x.y, x.y, q1); q2 = dot2(x.z, x.z, q2); q3 = dot2(x.w, x.w, q3); } };
    STAMP(31);
    VMC(16);
    patch_pad(R, l);
    VMC(0);
    STAMP(32);
    UNROLL for (int i = 0; i < 14; ++i) x_read(i / 7, i % 7);
    LGKM0();
    x_issue((j0 >> 1) ^ 1);
    sq(0); sq(1);
    asm volatile("" : "+v"(q0), "+v"(q1), "+v"(q2), "+v"(q3));
    BAR();
    STAMP(1);
    const rsrc_t rP = mk(P.wphi, 16 * 784 * 2);
    const unsigned ro = (64 * R.b_q + 16 * w + (l & 15)) * 896 + (56 * R.b_j + 4 * (l >> 4)) * 2;
    const v4u ones = v4u{0x3f803f80u, 0x3f803f80u, 0x3f803f80u, 0x3f803f80u};
    f32x16 ck, cv;
    v4u pk0, pk1;
    kv_gemm(l, j0, [&](int s, const v4u& k, const v4u& b) {
      if (s == 0) { MF32z(ck, ones, k); MF32z(cv, ones, b); } else if (s < 7) { MF32(ck, ones, k); MF32(cv, ones, b); }
      if (s == 8) asm volatile("" : "+v"(ck), "+v"(cv));
      if (s == 6) VMC(0);
      if (s >= 6 && s < 13) { x_read(2, s - 6); x_read(3, s - 6); }
      if (s == 13) {
        sts4(L_CEX + (w * 3 + (l >> 5)) * 128 + (l & 31) * 4, (l >> 5) ? cv[0] : ck[0]);
        const unsigned a = L_T + (w & 1) * 28672 + R.a_p * 1792 + l * 16;
        pk0 = lds16(a); pk1 = lds16(a + 1024);
        BAR();
      }
      if (s >= 13 && s < 20) stage_w1(P, R, wv0, 0, 7 * w + s - 13, L_WQ);
      else if (s == 20) { if ((w & 1) * 64 + l < 98) dma16(rP, ((w & 1) * 64 + l) * 16, R.a_h * 1568, L_WPHI + (w & 1) * 1024); }
      else if (s > 20 && s < 25) asm volatile("buffer_load_dwordx2 %0, %1, %2, 0 offen" : "=&v"(res[s - 21]) : "v"((s == 24 && l >= 32) ? OOB : ro + 32 * (s - 21)), "s"(rX) : "memory");
      else if (s == 25) {
        if (w < 2) { if (l < 49) touch(P.wo, (98 * R.rr + 49 * w + l) * 128); }
        else if (l < 36) ws_touch(P, R, PRETOUCH ? 36 * (w - 2) + l : 0, 0);
      } else if (s == 26) a_touch(P, R, l, 0);
      else if (s == 27) gst16<SC1>((w >> 1) ? pk1 : pk0, rWS, (w >> 1) && l >= 48 ? OOB : l * 16, WS_WPK + R.a_h * 86016 + (w & 1) * 28672 + R.a_p * 1792 + (w >> 1) * 1024);
    });
    sq(2); sq(3);
    ssq = (q0 + q1) + (q2 + q3);
    csk = colsum_all(l, 0); csv = colsum_all(l, 1);
    FENCE2(kT, vv);
    asm volatile("s_waitcnt vmcnt(3)" : "+v"(res[0]), "+v"(res[1]), "+v"(res[2]), "+v"(res[3])::"memory");
  }

  _Pragma("unroll 1") for (unsigned pair = 0; pair < 2; ++pair) {
    const u64 tag0 = nonce + 1 + 6 * pair;
    v4u phq[4]; float spq;
    {
      const Role R(c0, w0);
      const unsigned l = opq(lane) & 63, h = l >> 5, w = R.w, par = R.par;
      STAMP(2 + 14 * pair);
      float mean;
      {
        const float sx = par ? kT[0] : kT[12];
        auto r = __builtin_amdgcn_permlane32_swap(asu(sx), asu(sx), false, false);
        mean = asf(par ? r[0] : r[1]) * (1.f / 448.f);
      }
      const float var = fmaxf(sum32(ssq) * (1.f / 448.f) - mean * mean, 0.f);
      const float sig = __builtin_amdgcn_sqrtf(var + 1e-5f), rstd = __builtin_amdgcn_rcpf(sig);
      v4u xa;
      {
        const unsigned mh = pk2(-mean, -mean), ml = pk2(-mean - lo16(mh), -mean - lo16(mh));
        const unsigned sh = pk2(sig, sig), sl = pk2(sig, sig - lo16(sh));
        xa = v4u{h ? 0u : mh, h ? 0u : ml, h ? 0u : sl, 0u};
      }
      {
        const unsigned pd = par ? 0 : 28;
        const unsigned one = (l & 31) == pd ? 0x00003f80u : ((l & 31) == pd + 1 ? 0x3f800000u : 0u);
        const v4u a1 = v4u{csk, csk, 0u, 0u}, a2 = v4u{csv, csv, one, 0u};
        nMF32(kT, a1, xa); nMF32(vv, xa, a2);
      }
      BAR();
      v4u wp[2];
      {
        const int q4 = (l & 15) >> 2, fq = 4 * ((l >> 4) & 1) + (l & 3);
        UNROLL for (int s = 0; s < 2; ++s) {
          v2u t2[2];
          UNROLL for (int t = 0; t < 2; ++t) {
            const int d0 = 16 * s + 8 * t + 4 * (int)h - 4 * (int)par;
            const bool ok = d0 >= 0 && d0 < 28;
            const v2u r = tr8(L_WPHI + ((ok ? d0 : 0) + q4) * 56 + (fq < 7 ? fq : 0) * 8);
            const bool keep = ok && (l & 31) < 28;
            t2[t] = v2u{keep ? r.x : 0u, keep ? r.y : 0u};
          }
          wp[s] = cat(t2[0], t2[1]);
        }
      }
      const float sc = rstd * 1.44269504088896f;
      {
        FENCE2(kT, vv);
        f32x16 pk;
        const v4u k0 = pack8(kT, 0), k1 = pack8(kT, 1);
        nMF32z(pk, wp[0], k0); nMF32(pk, wp[1], k1);
        sts16(L_TOP + w * 2048 + l * 16, pack8(vv, 0)); sts16(L_TOP + w * 2048 + 1024 + l * 16, pack8(vv, 1));
        FENCE1(pk);
        float ep[16], en[16], sp, sn;
        dual_exp(pk, h, sc, ep, en, sp, sn);
        const float fp = rstd * __builtin_amdgcn_rcpf(sp), fn = rstd * __builtin_amdgcn_rcpf(sn);
        const unsigned tok = l & 31, sz = (tok & 7) | ((((tok >> 1) ^ (tok >> 3)) & 1) << 3), pb = L_MISC + w * 4096 + tok * 128;
        UNROLL for (int G = 0; G < 4; ++G) {
          sts8(pb + 8 * ((2 * G + h) ^ sz), v2u{pk2(ep[4 * G] * fp, ep[4 * G + 1] * fp), pk2(ep[4 * G + 2] * fp, ep[4 * G + 3] * fp)});
          sts8(pb + 8 * ((8 + 2 * G + h) ^ sz), v2u{pk2(en[4 * G] * fn, en[4 * G + 1] * fn), pk2(en[4 * G + 2] * fn, en[4 * G + 3] * fn)});
        }
      }
      BAR();
      {
        const unsigned g = l >> 4, q4 = (l & 15) >> 2, U = (w >> 1) * 8 + 4 * (w & 1) + (l & 3);
        const unsigned t0 = 16 * (g >> 1) + 4 * (g & 1) + q4, lo3 = 4 * (g & 1) + q4;
        const unsigned p0 = opq(L_MISC + t0 * 128 + 8 * (U ^ (lo3 | (((q4 >> 1) & 1) << 3)))), p1 = opq(L_MISC + (t0 + 8) * 128 + 8 * (U ^ (lo3 | ((((q4 >> 1) & 1) ^ 1) << 3))));
        const unsigned vb = opq(L_TOP + (g >> 1) * 1024 + ((g & 1) * 32 + (l & 15)) * 16);
        v4u a[4], b0[4], b1[4];
        UNROLL for (int i = 0; i < 4; ++i) { a[i] = tr16(p0 + i * 4096, p1 + i * 4096); b0[i] = lds16(vb + i * 2048); b1[i] = lds16(vb + i * 2048 + 256); }
        f32x4 d0, d1;
        MF16z(d0, a[0], b0[0]); MF16z(d1, a[0], b1[0]);
        UNROLL for (int i = 1; i < 4; ++i) { MF16(d0, a[i], b0[i]); MF16(d1, a[i], b1[i]); }
        asm volatile("s_nop 7" : "+v"(d0), "+v"(d1));
        auto r0 = __builtin_amdgcn_permlane32_swap(pk2(d0[0], d0[1]), pk2(d1[0], d1[1]), false, false);
        auto r1 = __builtin_amdgcn_permlane32_swap(pk2(d0[2], d0[3]), pk2(d1[2], d1[3]), false, false);
        gst16<SC1>(v4u{r0[0], r1[0], r0[1], r1[1]}, rWS, ((g & 1) * 32 + (l >> 5) * 16 + (l & 15)) * 16, WS_KVP + pair * MiB + (R.a_h * 16 + R.a_p) * 4096 + w * 1024);
      }
      STAMP(3 + 14 * pair);
      {
        f32x16 qT;
        v4u wq[5];
        unsigned a;
        const unsigned wl = L_WQ + (56 * h + ((l & 15) >> 2)) * 64 + ((l >> 4) & 1) * 32 + (l & 3) * 8;
        auto rd = [&](int s) { if (s % 7 == 0) a = opq(wl + ((s / 7) ^ R.j0) * 7168); wq[s % 5] = tr16(a + rhoA(s % 7, 0) * 64, a + rhoA(s % 7, 1) * 64); };
        const rsrc_t rWO = mk(P.wo, 448 * 448 * 2);
        const unsigned ov0 = wo_voff(l, 0), ov1 = wo_voff(l, 1);
        rd(0); rd(1); rd(2); rd(3);
        SB();
        UNROLL for (int s = 0; s < 28; ++s) {
          if (s == 0) { MF32z(qT, wq[0], xf[0][0]); } else { MF32(qT, wq[s % 5], xf[s / 7][s % 7]); }
          if (s + 4 < 28) rd(s + 4);
          if (s & 1) dma16(rWO, s < 14 ? ov0 : ov1, (14 * w + (s >> 1)) * 8 * 896 + 112 * R.b_j, L_WO + (14 * w + (s >> 1)) * 1024);
          if (s == QMID) {
            VMC((QMID + 1) / 2);
            asm volatile("s_barrier" ::: "memory");
            if (w == 1 && l < 16) post(rWS, WS_MB + (l * 16 + R.a_h) * 512 + R.a_p * 8, tag0);
          }
          SB();
        }
        if (pair == 0) {
          a_touch(P, R, l, 4);
          const v4u ones = v4u{0x3f803f80u, 0x3f803f80u, 0x3f803f80u, 0x3f803f80u};
          const unsigned aq = opq(wl + w * 7168);
          f32x16 cq;
          UNROLL for (int i = 0; i < 7; ++i) { const v4u t = tr16(aq + i * 512, aq + i * 512 + 256); if (i == 0) { nMF32z(cq, ones, t); } else { MF32(cq, ones, t); } }
          if (w < 2) gst16<SC1>(lds16(L_WQ + R.a_p * 1792 + w * 1024 + l * 16), rWS, w && l >= 48 ? OOB : l * 16, WS_WPK + R.a_h * 86016 + 57344 + R.a_p * 1792 + w * 1024);
          FENCE1(cq);
          if (h == 0) sts4(L_CEX + (w * 3 + 2) * 128 + l * 4, cq[0]);
          BAR();
          csq = colsum_all(l, 2);
        }
        const v4u a0 = v4u{csq, csq, 0u, 0u};
        nMF32(qT, a0, xa);
        FENCE1(qT);
        f32x16 pq;
        const v4u q0 = pack8(qT, 0), q1 = pack8(qT, 1);
        nMF32z(pq, wp[0], q0); nMF32(pq, wp[1], q1);
        FENCE1(pq);
        float ep[16], en[16], sn;
        dual_exp(pq, h, sc, ep, en, spq, sn);
        const float fn = spq * __builtin_amdgcn_rcpf(sn);
        UNROLL for (int s = 0; s < 2; ++s) {
          phq[s] = v4u{pk2(ep[8 * s], ep[8 * s + 1]), pk2(ep[8 * s + 2], ep[8 * s + 3]), pk2(ep[8 * s + 4], ep[8 * s + 5]), pk2(ep[8 * s + 6], ep[8 * s + 7])};
          phq[2 + s] = v4u{pk2(en[8 * s] * fn, en[8 * s + 1] * fn), pk2(en[8 * s + 2] * fn, en[8 * s + 3] * fn), pk2(en[8 * s + 4] * fn, en[8 * s + 5] * fn), pk2(en[8 * s + 6] * fn, en[8 * s + 7] * fn)};
        }
        asm volatile("" : "+v"(phq[0]), "+v"(phq[1]), "+v"(phq[2]), "+v"(phq[3]), "+v"(spq));
      }
      if (pair == 0) a_issue_all(P, R, l, 0);
      STAMP(4 + 14 * pair);
      if (w == 0 && l < 16) poll(rWS, WS_MB + (R.a_p * 16 + R.a_h) * 512 + l * 8, tag0);
      BAR();
    }
    STAMP(5 + 14 * pair);
    {
      const Role R(c0, w0);
      const unsigned l = opq(lane) & 63, h = l >> 5, w = R.w, par = R.par;
      v4u pt[16];
      UNROLL for (int i = 0; i < 16; ++i) { pt[i] = gld16<SC1>(rWS, l * 16, WS_KVP + pair * MiB + (R.a_h * 16 + i) * 4096 + w * 1024); SB(); }
      {
        v4u sel;
        UNROLL for (int e = 0; e < 4; ++e) {
          const unsigned r0 = ((2 * e) & 3) + 8 * ((2 * e) >> 2) + 4 * h;
          sel[e] = (l & 31) == r0 ? 0x00003f80u : ((l & 31) == r0 + 1 ? 0x3f800000u : 0u);
        }
        f32x16 t;
        nMF32z(t, sel, pt[0]);
        UNROLL for (int i = 1; i < 16; ++i) MF32(t, sel, pt[i]);
        FENCE1(t);
        sts16(L_MISC + w * 1024 + l * 16, pack8(t, 0));
      }
      BAR();
      f32x16 n;
      v4u kf[4];
      const unsigned kb = opq(L_MISC + l * 16);
      UNROLL for (int i = 0; i < 4; ++i) kf[i] = lds16(kb + i * 1024);
      MF32z(n, kf[0], phq[0]); MF32(n, kf[1], phq[1]); MF32(n, kf[2], phq[2]); MF32(n, kf[3], phq[3]);
      FENCE1(n);
      const float ds = par ? n[0] + n[1] : n[12] + n[13];
      auto r = __builtin_amdgcn_permlane32_swap(asu(ds), asu(ds), false, false);
      const float den = asf(par ? r[0] : r[1]);
      const float inv = __builtin_amdgcn_rcpf(den + 1e-6f * spq);
      v2u o[4];
      UNROLL for (int G = 0; G < 4; ++G) o[G] = v2u{pk2(n[4 * G] * inv, n[4 * G + 1] * inv), pk2(n[4 * G + 2] * inv, n[4 * G + 3] * inv)};
      const unsigned Pp = R.a_h >> 1;
      const unsigned so = WS_OF + pair * 2 * MiB + (8 * R.a_p + 2 * w) * 14336 + 7 * (Pp >> 2) * 1024 + (Pp & 3) * 256;
      const unsigned vo = ((l >> 4) & 1) * 14336 + (l & 15) * 16;
      const bool full2 = h == par, useB = par && !h;
      gst16<SC1>(useB ? cat(o[2], o[3]) : cat(o[0], o[1]), rWS, vo + (par ? (h ? 4 : 6) : (h ? 2 : 0)) * 1024, so);
      gst16<SC1>(cat(o[2], o[3]), rWS, full2 ? vo + (par ? 5 : 1) * 1024 : OOB, so);
      gst8<SC1>(par ? o[1] : o[2], rWS, full2 ? OOB : vo + 3 * 1024 + 8 * par, so);
      STAMP(6 + 14 * pair);
      if (pair == 0) a_read<0, 9>(R, l);
      DRAIN();
      BAR();
      if (w == 1 && l < 16) post(rWS, WS_MB + 131072 + ((2 * R.a_p + (l >> 3)) * 8 + (l & 7)) * 512 + R.a_h * 8, tag0 + 1);
      if (pair == 0) a_issue_all(P, R, l, 9);
    }
    {
      const Role R(c0, w0);
      const unsigned l = opq(lane) & 63, g = l >> 4, q4 = (l & 15) >> 2, w = R.w;
      f32x4 acc[4];
      {
        constexpr int PRE = 8, LEAD = 12;
        const unsigned f = ((q4 >> 1) & 1) | ((g & 1) << 1), wb = L_WO + (56 * g + q4) * 128 + (l & 3) * 8;
        unsigned ba[4];
        UNROLL for (int a = 0; a < 4; ++a) ba[a] = opq(wb + 32 * (a ^ f));
        v4u wf[PRE][4];
        auto rd = [&](int s, int a) { wf[s % PRE][a] = tr16(ba[a] + (224 * (s / 7) + rhoO(s % 7, 0)) * 128, ba[a] + (224 * (s / 7) + rhoO(s % 7, 1)) * 128); };
        UNROLL for (int s = 0; s < PRE; ++s) UNROLL for (int a = 0; a < 4; ++a) rd(s, a);
        UNROLL for (int a = 0; a < 4; ++a) acc[a] = f32x4{lo16(res[a].x), hi16(res[a].x), lo16(res[a].y), hi16(res[a].y)};
        asm volatile("" : "+v"(acc[0]), "+v"(acc[1]), "+v"(acc[2]), "+v"(acc[3]));
        if (w == 0 && l < 16) poll(rWS, WS_MB + 131072 + (R.b_q * 8 + R.b_j) * 512 + l * 8, tag0 + 1);
        BAR();
        STAMP(7 + 14 * pair);
        v4u of[14];
        auto ld = [&](int s) { of[s] = gld16<SC1>(rWS, l * 16 + (s & 3) * 1024, WS_OF + pair * 2 * MiB + (4 * R.b_q + w) * 14336 + (s >> 2) * 4096); };
        UNROLL for (int s = 0; s < LEAD; ++s) { ld(s); SB(); }
        UNROLL for (int s = 0; s < 14; ++s)
          UNROLL for (int a = 0; a < 4; ++a) { MF16(acc[a], wf[s % PRE][a], of[s]); if (a == 0 && s + LEAD < 14) ld(s + LEAD); if (s + PRE < 14) rd(s + PRE, a); SB(); }
        FENCE4(acc);
      }
      UNROLL for (int e = 0; e < 4; ++e) acc[3][e] = l >= 32 ? 0.f : acc[3][e];
      float s1 = 0.f, s2 = 0.f;
      UNROLL for (int a = 0; a < 4; ++a) UNROLL for (int e = 0; e < 4; ++e) { s1 += acc[a][e]; s2 = __builtin_fmaf(acc[a][e], acc[a][e], s2); }
      s1 = sum32(sum16(s1)); s2 = sum32(sum16(s2));
      const unsigned tg = (unsigned)tag0 + 2;
      const unsigned g3 = WS_G3 + pair * 262144 + R.b_q * 8192 + w * 256;
      gst16<SC1>(v4u{asu(s1), tg, asu(s2), tg}, rWS, l < 16 ? l * 16 : OOB, g3 + R.b_j * 1024);
      UNROLL for (int a = 0; a < 4; ++a) res[a] = v2u{pk2(acc[a][0], acc[a][1]), pk2(acc[a][2], acc[a][3])};
      STAMP(8 + 14 * pair);
      if (pair == 0) {
        BAR();
        a_read<9, 18>(R, l);
        a_issue_own(P, R, l, 18, 9);
        if (l < 49) touch((w >> 1) ? P.hi : P.hr, (98 * R.rr + 49 * (w & 1) + l) * 128);
      } else __builtin_amdgcn_s_sleep(S3SLEEP);
      v4u ga, gb;
      _Pragma("unroll 1") for (unsigned spin = 0; spin < 0x10000; ++spin) {
        ga = gld16<SC1>(rWS, (l & 15) * 16 + g * 2048, g3); gb = gld16<SC1>(rWS, (l & 15) * 16 + g * 2048 + 1024, g3);
        if (__all(ga.y == tg && ga.w == tg && gb.y == tg && gb.w == tg)) break;
        asm volatile("" ::: "memory");
      }
      STAMP(9 + 14 * pair);
      const float t1 = sum32(sum16(asf(ga.x) + asf(gb.x)));
      const float t2 = sum32(sum16(asf(ga.z) + asf(gb.z)));
      const float mean = t1 * (1.f / 448.f), var = fmaxf(t2 * (1.f / 448.f) - mean * mean, 0.f);
      const float rstd = __builtin_amdgcn_rsqf(var + 1e-5f), nb = -mean * rstd;
      const unsigned img = L_TOP + w * 2048;
      UNROLL for (int a = 0; a < 4; ++a)
        sts8(img + (l & 15) * 128 + 8 * ((4 * a + g) ^ (l & 15)),
             v2u{pk2(__builtin_fmaf(acc[a][0], rstd, nb), __builtin_fmaf(acc[a][1], rstd, nb)), pk2(__builtin_fmaf(acc[a][2], rstd, nb), __builtin_fmaf(acc[a][3], rstd, nb))});
      UNROLL for (int n = 0; n < 2; ++n) {
        const unsigned T = 4 * n + g, o = T & 1, QS = T >> 1, t0 = 8 * o + q4;
        const v4u d = tr16(img + t0 * 128 + 8 * ((4 * QS + (l & 3)) ^ t0), img + (t0 + 4) * 128 + 8 * ((4 * QS + (l & 3)) ^ (t0 + 4)));
        const unsigned quad = 4 * QS + q4, cq = 56 * (R.b_j & 1) + 4 * quad;
        const unsigned tile = cq < 96 ? 2 * (cq >> 5) + ((cq >> 2) & 1) : 6, ii = (cq < 96 ? 4 * ((cq & 31) >> 3) : cq - 96) + (l & 3);
        gst16<SC1>(d, rWS, quad < 14 ? tile * 1024 + ((2 * (w & 1) + o) * 16 + ii) * 16 : OOB,
                   WS_XN + pair * 2 * MiB + (R.b_j >> 1) * 458752 + (2 * R.b_q + (w >> 1)) * 7168);
      }
      if (pair == 0) a_read<18, 27>(R, l);
      DRAIN();
      BAR();
      if (w == 1) post(rWS, WS_MB + 2 * 131072 + (l * 4 + (R.b_j >> 1)) * 512 + (2 * R.b_q + (R.b_j & 1)) * 8, tag0 + 3);
      if (pair == 0) a_issue_own(P, R, l, 27, 5);
      stage_h(P, R, l);
      if (w == 0) poll(rWS, WS_MB + 2 * 131072 + (R.d_t * 4 + R.d_j) * 512 + l * 8, tag0 + 3);
      BAR();
    }
    STAMP(10 + 14 * pair);
    {
      const Role R(c0, w0);
      const unsigned l = opq(lane) & 63, w = R.w;
      constexpr int NB = B1DIST / 7 + 1 + (B1DIST % 7 != 0);
      f32x4 acc[4][7];
      v4u b[NB][7];
      const unsigned xb = WS_XN + pair * 2 * MiB + R.d_j * 458752 + 16 * w * 7168;
      auto ldb = [&](int i) { const int s = i / 7, n = i % 7; b[s % NB][n] = gld16<SC1>(rWS, l * 16 + (n & 3) * 1024, xb + ((s + R.rot) & 15) * 7168 + (n >> 2) * 4096); };
      UNROLL for (int i = 0; i < B1DIST; ++i) { ldb(i); SB(); }
      UNROLL for (int s = 0; s < 16; ++s) {
        UNROLL for (int n = 0; n < 7; ++n) {
          UNROLL for (int m = 0; m < 4; ++m) {
            if (s == 0) { MF16Az(acc[m][n], b[0][n], m); } else { MF16A(acc[m][n], b[s % NB][n], 4 * s + m); }
            if (m == 0 && 7 * s + n + B1DIST < 112) ldb(7 * s + n + B1DIST);
          }
          SB();
        }
        if (s == 1 && pair == 0) a_read<27, 32>(R, l);
      }
      asm volatile("s_nop 7");
      PIN7(acc[0]); PIN7(acc[1]); PIN7(acc[2]); PIN7(acc[3]);
      STAMP(11 + 14 * pair);
      const unsigned r1 = opq(L_T + w * 14336 + l * 16), r2 = opq(L_T + (w ^ 1) * 14336 + l * 16), r3 = opq(L_T + (w ^ 3) * 14336 + l * 16);
      UNROLL for (int n = 0; n < 7; ++n) sts16(r1 + (7 + n) * 1024, __builtin_bit_cast(v4u, acc[3][n]));
      UNROLL for (int n = 0; n < 7; ++n) sts16(r1 + n * 1024, __builtin_bit_cast(v4u, acc[1][n]));
      BAR();
      UNROLL for (int n = 0; n < 7; ++n) acc[2][n] += __builtin_bit_cast(f32x4, lds16(r2 + (7 + n) * 1024));
      UNROLL for (int n = 0; n < 7; ++n) sts16(r2 + (7 + n) * 1024, __builtin_bit_cast(v4u, acc[2][n]));
      acc[0][6] += __builtin_bit_cast(f32x4, lds16(r2 + 6 * 1024));
      sts16(r2 + 6 * 1024, __builtin_bit_cast(v4u, acc[0][6]));
      UNROLL for (int n = 0; n < 6; ++n) acc[0][n] += __builtin_bit_cast(f32x4, lds16(r2 + n * 1024));
      BAR();
      UNROLL for (int n = 0; n < 7; ++n) acc[0][n] += __builtin_bit_cast(f32x4, lds16(r3 + (7 + n) * 1024));
      const f32x4 other = acc[2][6] + __builtin_bit_cast(f32x4, lds16(r3 + 6 * 1024));
      const unsigned sg = (w >> 1) ? 0x80008000u : 0u;
      v2u o[7];
      UNROLL for (int n = 0; n < 7; ++n) o[n] = v2u{pk2(acc[0][n][0], acc[0][n][1]) ^ sg, pk2(acc[0][n][2], acc[0][n][3]) ^ sg};
      const unsigned so = WS_PQ + pair * 4 * MiB + (2 * R.d_t + (w & 1)) * 28672 + 7 * R.d_j * 1024;
      UNROLL for (int u = 0; u < 3; ++u) gst16<SC1>(cat(o[2 * u], o[2 * u + 1]), rWS, l * 16 + u * 1024, so + (w >> 1) * 3072);
      if (w < 2) gst16<SC1>(cat(o[6], v2u{pk2(other[0], other[1]) ^ 0x80008000u, pk2(other[2], other[3]) ^ 0x80008000u}), rWS, l * 16, so + 6144);
      STAMP(12 + 14 * pair);
      DRAIN();
      BAR();
      if (w == 1 && l < 8) post(rWS, WS_MB + 3 * 131072 + ((R.d_t >> 1) * 8 + l) * 512 + (4 * (R.d_t & 1) + R.d_j) * 8, tag0 + 4);
      if (pair == 0) {
        if (PRETOUCH && w >= 2 && l < 36) ws_touch(P, R, 36 * (w - 2) + l, 1);
        FOR_3WAVES(56, n) dma16c(rWS, l * 16, WS_WPK + R.a_h * 86016 + n * 1024, L_T + n * 1024);
      }
    }
    {
      const Role R(c0, w0);
      const unsigned l = opq(lane) & 63, g = l >> 4, e16 = l & 15, f = (e16 >> 1) & 7, w = R.w;
      f32x4 acc[4];
      {
        constexpr int PRE = 7, LEAD = 12;
        unsigned hb[2][4], hm[2][4];
        UNROLL for (int k = 0; k < 4; ++k) {
          hb[0][k] = opq(((2 * k + g) >> 3) * 7168 + e16 * 128 + 16 * (((2 * k + g) & 7) ^ f)); hb[1][k] = opq(hb[0][k] + L_HI);
          hm[0][k] = opq(e16 * 128 + 16 * ((2 * k + (g >> 1)) ^ f) + 8 * (g & 1)); hm[1][k] = opq(hm[0][k] + L_HI);
        }
        v4u wf[PRE][4];
        auto rd = [&](int s, int a) {
          const int j = s / 7, i = s % 7, c = 14 * j + (i < 6 ? 4 * (i % 3) : 12);
          if (i < 6) wf[s % PRE][a] = lds16(hb[i / 3][(c & 7) >> 1] + (c >> 3) * 7168 + a * 2048);
          else wf[s % PRE][a] = cat(lds8(hm[0][(c & 7) >> 1] + (c >> 3) * 7168 + a * 2048), lds8(hm[1][(c & 7) >> 1] + (c >> 3) * 7168 + a * 2048));
        };
        UNROLL for (int s = 0; s < PRE; ++s) { UNROLL for (int a = 0; a < 4; ++a) { rd(s, a); asm volatile("" ::: "memory"); } }
        UNROLL for (int a = 0; a < 4; ++a) acc[a] = f32x4{lo16(res[a].x), hi16(res[a].x), lo16(res[a].y), hi16(res[a].y)};
        asm volatile("" : "+v"(acc[0]), "+v"(acc[1]), "+v"(acc[2]), "+v"(acc[3]));
        if (w == 0 && l < 8) poll(rWS, WS_MB + 3 * 131072 + (R.b_q * 8 + R.b_j) * 512 + l * 8, tag0 + 4);
        BAR();
        STAMP(13 + 14 * pair);
        v4u pf[28];
        auto ld = [&](int s) { pf[s] = gld16<SC1>(rWS, l * 16 + (s & 3) * 1024, WS_PQ + pair * 4 * MiB + (4 * R.b_q + w) * 28672 + (s >> 2) * 4096); };
        UNROLL for (int s = 0; s < LEAD; ++s) { ld(s); SB(); }
        UNROLL for (int s = 0; s < 28; ++s)
          UNROLL for (int a = 0; a < 4; ++a) { MF16(acc[a], wf[s % PRE][a], pf[s]); if (a == 0 && s + LEAD < 28) ld(s + LEAD); if (s + PRE < 28) rd(s + PRE, a); SB(); }
        FENCE4(acc);
      }
      UNROLL for (int a = 0; a < 4; ++a) res[a] = v2u{pk2(acc[a][0], acc[a][1]), pk2(acc[a][2], acc[a][3])};
      if (pair == 1) {
        const rsrc_t rO = mk(P.out, 2048 * 448 * 2);
        const unsigned ro = (64 * R.b_q + 16 * w + e16) * 896 + (56 * R.b_j + 4 * g) * 2;
        UNROLL for (int a = 0; a < 4; ++a) gst8<0>(res[a], rO, (a == 3 && l >= 32) ? OOB : ro + 32 * a, 0);
        UNROLL for (int Js = 0; Js < 4; ++Js)
          asm volatile("" : "=v"(xf[Js][0]), "=v"(xf[Js][1]), "=v"(xf[Js][2]), "=v"(xf[Js][3]), "=v"(xf[Js][4]), "=v"(xf[Js][5]), "=v"(xf[Js][6]));
        asm volatile("" : "=v"(kT), "=v"(vv), "=v"(ssq));
      } else {
        const unsigned so = WS_X2 + (2 * R.b_q + (w >> 1)) * 28672 + 7 * (R.b_j >> 1) * 1024 + (R.b_j & 1) * 512 + (w & 1) * 256;
        gst16<SC1>(cat(res[0], res[1]), rWS, g * 1024 + e16 * 16, so);
        gst16<SC1>(cat(res[2], res[3]), rWS, l < 32 ? (4 + g) * 1024 + e16 * 16 : OOB, so);
        gst8<SC1>(res[2], rWS, l < 32 ? OOB : 6 * 1024 + e16 * 16 + (g & 1) * 8, so);
        float s2 = 0.f, s3 = 0.f;
        UNROLL for (int a = 0; a < 3; ++a) UNROLL for (int e = 0; e < 4; ++e) s2 = __builtin_fmaf(acc[a][e], acc[a][e], s2);
        UNROLL for (int e = 0; e < 4; ++e) s3 = __builtin_fmaf(acc[3][e], acc[3][e], s3);
        s2 = sum32(sum16(s2 + (l < 32 ? s3 : 0.f)));
        __builtin_amdgcn_raw_buffer_store_b32(asu(s2), rWS, l < 32 ? l * 4 : OOB, WS_SSQ + (4 * R.b_q + w) * 1024 + R.b_j * 128, SC1);
        STAMP(14);
        DRAIN();
        BAR();
        if (w == 1 && l < 16) post(rWS, WS_MB + 4 * 131072 + ((R.b_q >> 1) * 16 + l) * 512 + (8 * (R.b_q & 1) + R.b_j) * 8, tag0 + 5);
        FOR_3WAVES(28, n) dma16c(rWS, l * 16, WS_WPK + R.a_h * 86016 + 57344 + n * 1024, L_WQ + n * 1024);
        if (w == 0 && l < 16) poll(rWS, WS_MB + 4 * 131072 + (R.a_p * 16 + R.a_h) * 512 + l * 8, tag0 + 5);
        BAR();
        STAMP(15);
        constexpr int LEAD = 12;
        auto ld = [&](int s) { xf[s / 7][s % 7] = gld16<SC1>(rWS, l * 16 + ((s % 7) & 3) * 1024, WS_X2 + (4 * R.a_p + w) * 28672 + ((s / 7) ^ R.j0) * 7168 + ((s % 7) >> 2) * 4096); };
        UNROLL for (int s = 0; s < LEAD; ++s) { ld(s); SB(); }
        unsigned sq[4];
        kv_gemm(l, R.j0, [&](int s, const v4u&, const v4u&) {
          if (s + LEAD < 28) ld(s + LEAD);
          else if (s + LEAD < 32) sq[s + LEAD - 28] = __builtin_amdgcn_raw_buffer_load_b32(rWS, ((l >> 4) & 1) * 1024 + (l >> 5) * 512 + (l & 15) * 4 + (s + LEAD - 28) * 128, WS_SSQ + (8 * R.a_p + 2 * w) * 1024, SC1);
        });
        ssq = (asf(sq[0]) + asf(sq[1])) + (asf(sq[2]) + asf(sq[3]));
        FENCE2(kT, vv);
      }
    }
  }
  if (c0 == 0 && w0 == 0 && lane == 0) {
    const u64 nn = nonce + 16;
    gst8<SC1>(v2u{(unsigned)nn, (unsigned)(nn >> 32)}, rWS, 0, WS_NONCE);
  }
  STAMP(28);
  if (c0 == 0) { u64 te, t; RT(te); const u64 d = ptk_ > pt0_ ? ptk_ - pt0_ : 0; do { RT(t); } while (t - te < d && t - te < 20000); }
  asm volatile("Lfused_end:");
}

}

extern "C" void kernel_launch(void* const* d_in, const int* in_sizes, int n_in, void* d_out, int out_size, void* d_ws, size_t ws_size,
                              hipStream_t stream) {
  fused::Params p;
  p.x = d_in[0]; p.wqkv = d_in[1]; p.wphi = d_in[2]; p.wo = d_in[3];
  p.cr = d_in[5]; p.ci = d_in[6]; p.hr = d_in[7]; p.hi = d_in[8];
  p.out = d_out; p.ws = d_ws;
  fused::fused_fwd<<<256, 256, 0, stream>>>(p);
}
```

```cpp
#include <hip/hip_runtime.h>
#include <hip/hip_bf16.h>
#include <stdint.h>

namespace fused {

typedef unsigned v4u __attribute__((ext_vector_type(4)));
typedef unsigned v2u __attribute__((ext_vector_type(2)));
typedef float f32x2 __attribute__((ext_vector_type(2)));
typedef float f32x4 __attribute__((ext_vector_type(4)));
typedef float f32x16 __attribute__((ext_vector_type(16)));
typedef __bf16 bf16x2 __attribute__((ext_vector_type(2)));
typedef __bf16 bf16x8 __attribute__((ext_vector_type(8)));
typedef short s16x4 __attribute__((ext_vector_type(4)));
typedef __amdgpu_buffer_rsrc_t rsrc_t;
typedef unsigned long long u64;
#define LAS __attribute__((address_space(3)))
#define DI __device__ __forceinline__

constexpr unsigned MiB = 1u << 20;
constexpr unsigned WS_NONCE = 0;
constexpr unsigned WS_MB = 4096;
constexpr unsigned WS_G3 = 1 * MiB;
constexpr unsigned WS_KVP = 2 * MiB;
constexpr unsigned WS_OF = 4 * MiB;
constexpr unsigned WS_XN = 8 * MiB;
constexpr unsigned WS_PQ = 12 * MiB;
constexpr unsigned WS_X2 = 20 * MiB;
constexpr unsigned WS_SSQ = 22 * MiB;
constexpr unsigned WS_WPK = 22 * MiB + 262144;
constexpr unsigned WS_STAMP = 23 * MiB + 786432;
constexpr unsigned WS_BYTES = 24 * MiB;
constexpr unsigned OOB = 0x7FFFFFF0u;
constexpr int SC1 = 16;

constexpr unsigned L_WQ = 0, L_WO = 28672, L_MISC = 86016, L_SMALL = 102400, L_T = 106496, L_TOP = 155648;
constexpr unsigned L_WPHI = L_SMALL, L_CEX = L_SMALL + 1664, L_STAMP = L_SMALL + 3328, L_JUNK = L_SMALL + 3840;
constexpr unsigned L_HI = 50176;
constexpr unsigned LDS_BYTES = 163840;

struct Params {
  const void *x, *wqkv, *wphi, *wo, *cr, *ci, *hr, *hi;
  void *out, *ws;
};

#define MF16(c, a, b) asm volatile("v_mfma_f32_16x16x32_bf16 %0, %1, %2, %0" : "+v"(c) : "v"(a), "v"(b))
#define MF16z(c, a, b) asm volatile("v_mfma_f32_16x16x32_bf16 %0, %1, %2, 0" : "=&v"(c) : "v"(a), "v"(b))
#define nMF16(c, a, b) asm volatile("s_nop 1\n\tv_mfma_f32_16x16x32_bf16 %0, %1, %2, %0" : "+v"(c) : "v"(a), "v"(b))
#define MF32(c, a, b) asm volatile("v_mfma_f32_32x32x16_bf16 %0, %1, %2, %0" : "+v"(c) : "v"(a), "v"(b))
#define MF32z(c, a, b) asm volatile("v_mfma_f32_32x32x16_bf16 %0, %1, %2, 0" : "=&v"(c) : "v"(a), "v"(b))
#define nMF32(c, a, b) asm volatile("s_nop 1\n\tv_mfma_f32_32x32x16_bf16 %0, %1, %2, %0" : "+v"(c) : "v"(a), "v"(b))
#define nMF32z(c, a, b) asm volatile("s_nop 1\n\tv_mfma_f32_32x32x16_bf16 %0, %1, %2, 0" : "=&v"(c) : "v"(a), "v"(b))
#define MF16A(c, a, R) asm volatile("v_mfma_f32_16x16x32_bf16 %0, %1, a[%c2:%c3], %0" : "+v"(c) : "v"(a), "i"(4 * (R)), "i"(4 * (R) + 3))
#define MF16Az(c, a, R) asm volatile("v_mfma_f32_16x16x32_bf16 %0, %1, a[%c2:%c3], 0" : "=&v"(c) : "v"(a), "i"(4 * (R)), "i"(4 * (R) + 3))
#define FENCE1(c) asm volatile("s_nop 11" : "+v"(c))
#define FENCE2(c, d) asm volatile("s_nop 11" : "+v"(c), "+v"(d))
#define FENCE4(c) asm volatile("s_nop 7" : "+v"(c[0]), "+v"(c[1]), "+v"(c[2]), "+v"(c[3]))
#define PIN7(c) asm volatile("" : "+v"(c[0]), "+v"(c[1]), "+v"(c[2]), "+v"(c[3]), "+v"(c[4]), "+v"(c[5]), "+v"(c[6]))
#define A_LDS_RD(R, addr, off) asm volatile("ds_read_b128 a[%c0:%c1], %2 offset:%c3" ::"i"(4 * (R)), "i"(4 * (R) + 3), "v"(addr), "i"(off) : "memory")
DI float asf(unsigned u) { return __builtin_bit_cast(float, u); }
DI unsigned asu(float f) { return __builtin_bit_cast(unsigned, f); }
DI unsigned pk2(float a, float b) { return __builtin_bit_cast(unsigned, __builtin_convertvector(f32x2{a, b}, bf16x2)); }
DI float lo16(unsigned u) { return asf(u << 16); }
DI float hi16(unsigned u) { return asf(u & 0xffff0000u); }
DI float dot2(unsigned a, unsigned b, float c) { return __builtin_amdgcn_fdot2_f32_bf16(__builtin_bit_cast(bf16x2, a), __builtin_bit_cast(bf16x2, b), c, false); }
DI v4u lds16(unsigned a) { return *(LAS const v4u*)(size_t)a; }
DI v2u lds8(unsigned a) { return *(LAS const v2u*)(size_t)a; }
DI float lds4(unsigned a) { return *(LAS const float*)(size_t)a; }
DI void sts16(unsigned a, v4u v) { *(LAS v4u*)(size_t)a = v; }
DI void sts8(unsigned a, v2u v) { *(LAS v2u*)(size_t)a = v; }
DI void sts4(unsigned a, float v) { *(LAS float*)(size_t)a = v; }
DI v2u tr8(unsigned a) { return __builtin_bit_cast(v2u, __builtin_amdgcn_ds_read_tr16_b64_v4i16((LAS s16x4*)(size_t)a)); }
DI v4u cat(v2u a, v2u b) { return v4u{a.x, a.y, b.x, b.y}; }
DI v4u tr16(unsigned a0, unsigned a1) { return cat(tr8(a0), tr8(a1)); }
template <int AUX> DI v4u gld16(rsrc_t r, unsigned v, unsigned s) { return __builtin_amdgcn_raw_buffer_load_b128(r, v, s, AUX); }
template <int AUX> DI v2u gld8(rsrc_t r, unsigned v, unsigned s) { return __builtin_amdgcn_raw_buffer_load_b64(r, v, s, AUX); }
template <int AUX> DI void gst16(v4u d, rsrc_t r, unsigned v, unsigned s) { __builtin_amdgcn_raw_buffer_store_b128(d, r, v, s, AUX); }
template <int AUX> DI void gst8(v2u d, rsrc_t r, unsigned v, unsigned s) { __builtin_amdgcn_raw_buffer_store_b64(d, r, v, s, AUX); }
DI rsrc_t mk(const void* p, unsigned bytes) { return __builtin_amdgcn_make_buffer_rsrc((void*)p, 0, bytes, 0x00020000); }
DI void set_m0(unsigned lds) { asm volatile("s_mov_b32 m0, %0\n\ts_nop 0" ::"s"(lds) : "memory"); }
template <int OFF> DI void dma(rsrc_t r, unsigned v, unsigned s) { asm volatile("buffer_load_dwordx4 %0, %1, %2 offen offset:%c3 lds" ::"v"(v), "s"(r), "s"(s), "i"(OFF) : "memory"); }
template <int OFF> DI void dmac(rsrc_t r, unsigned v, unsigned s) {
  asm volatile("buffer_load_dwordx4 %0, %1, %2 offen offset:%c3 sc1 lds" ::"v"(v), "s"(r), "s"(s), "i"(OFF) : "memory");
}
DI void dma16(rsrc_t r, unsigned v, unsigned s, unsigned lds) { set_m0(lds); dma<0>(r, v, s); }
DI void touch(const void* base, unsigned off) {
  asm volatile("s_mov_b32 m0, %1\n\ts_nop 0\n\tglobal_load_lds_dword %0, off" ::"v"((const char*)base + off), "s"(L_JUNK) : "memory");
}
#define VMC(n) asm volatile("s_waitcnt vmcnt(%c0)" ::"i"(n) : "memory")
#define BAR() asm volatile("s_waitcnt lgkmcnt(0)\n\ts_barrier" ::: "memory")
#define DRAIN() asm volatile("s_waitcnt vmcnt(0)" ::: "memory")
#define LGKM0() asm volatile("s_waitcnt lgkmcnt(0)" ::: "memory")
#define SB() __builtin_amdgcn_sched_barrier(0)
#define UNROLL _Pragma("unroll")
template <int OFF> DI void b_load(v4u& d, rsrc_t r, unsigned v, unsigned s) { asm volatile("buffer_load_dwordx4 %0, %1, %2, %3 offen offset:%c4 sc1" : "=v"(d) : "v"(v), "s"(r), "s"(s), "i"(OFF) : "memory"); }
template <int N> DI void b_wait(v4u& d) { asm volatile("s_waitcnt vmcnt(%c1)" : "+v"(d) : "i"(N)); }
template <int R, bool Z> DI void mf16a(f32x4& c, const v4u& a) { if constexpr (Z) { MF16Az(c, a, R); } else { MF16A(c, a, R); } }
template <int I> struct IC { static constexpr int v = I; };
template <int I, int N, class F> DI void static_for(F&& f) { if constexpr (I < N) { f(IC<I>{}); static_for<I + 1, N>(f); } }
DI unsigned opq(unsigned v) { asm volatile("" : "+v"(v)); return v; }
DI unsigned sopq(unsigned v) { asm volatile("" : "+s"(v)); return v; }
DI float sum32(float v) { auto r = __builtin_amdgcn_permlane32_swap(asu(v), asu(v), false, false); return asf(r[0]) + asf(r[1]); }
DI float sum16(float v) { auto r = __builtin_amdgcn_permlane16_swap(asu(v), asu(v), false, false); return asf(r[0]) + asf(r[1]); }
DI float max32(float v) { auto r = __builtin_amdgcn_permlane32_swap(asu(v), asu(v), false, false); return fmaxf(asf(r[0]), asf(r[1])); }
DI float min32(float v) { auto r = __builtin_amdgcn_permlane32_swap(asu(v), asu(v), false, false); return fminf(asf(r[0]), asf(r[1])); }

DI void post(rsrc_t ws, unsigned voff, u64 tag) {
  asm volatile("buffer_atomic_swap_x2 %0, %1, %2, 0 offen\n\ts_nop 1" ::"v"(tag), "v"(voff), "s"(ws) : "memory");
}
DI u64 poll_start(rsrc_t ws, unsigned voff) {
  u64 t;
  asm volatile("v_mov_b64 %0, 0\n\ts_nop 1\n\tbuffer_atomic_add_x2 %0, %1, %2, 0 offen sc0" : "=&v"(t) : "v"(voff), "s"(ws) : "memory");
  return t;
}
DI void poll_end(rsrc_t ws, unsigned voff, u64 tag, u64 t) {
  unsigned cnt = 0;
  asm volatile(
      "Lp%=:\n\ts_waitcnt vmcnt(0)\n\tv_cmp_eq_u64 vcc, %4, %1\n\ts_add_u32 %0, %0, 1\n\ts_cmp_eq_u64 vcc, exec\n\ts_cbranch_scc1 Ld%=\n\t"
      "v_mov_b64 %1, 0\n\ts_cmp_lt_u32 %0, 0x10000\n\ts_cbranch_scc0 Ld%=\n\tbuffer_atomic_add_x2 %1, %2, %3, 0 offen sc0\n\ts_branch Lp%=\n\tLd%=:"
      : "+s"(cnt), "+v"(t) : "v"(voff), "s"(ws), "s"(tag) : "vcc", "scc", "memory");
}
DI void poll(rsrc_t ws, unsigned voff, u64 tag) { poll_end(ws, voff, tag, poll_start(ws, voff)); }

#define CSTAMP(i) do {} while (0)
#define STAMP(i) do {} while (0)

DI constexpr int rhoA(int i, int t) { return i < 4 ? (t ? 16 + 4 * i : 4 * i) : (i < 6 ? (t ? 48 + 4 * (i - 4) : 32 + 4 * (i - 4)) : (t ? 44 : 40)); }
DI constexpr int rhoO(int i, int t) { return i == 0 ? (t ? 8 : 0) : i == 1 ? (t ? 24 : 16) : i == 2 ? (t ? 12 : 4) : i == 3 ? (t ? 32 : 20) : i == 4 ? (t ? 36 : 28) : i == 5 ? (t ? 52 : 44) : (t ? 48 : 40); }

DI v4u pack8(const f32x16& x, int s) { return v4u{pk2(x[8 * s], x[8 * s + 1]), pk2(x[8 * s + 2], x[8 * s + 3]), pk2(x[8 * s + 4], x[8 * s + 5]), pk2(x[8 * s + 6], x[8 * s + 7])}; }

DI void dual_exp(const f32x16& p, float keep, float sc, f32x2 (&ep)[8], f32x2 (&en)[8], float& sp, float& sn) {
  float mx, mn;
  asm("v_max3_f32 %0, %2, %3, %4\n\tv_min3_f32 %1, %2, %3, %4\n\tv_max3_f32 %0, %0, %5, %6\n\tv_min3_f32 %1, %1, %5, %6\n\t"
      "v_max3_f32 %0, %0, %7, %8\n\tv_min3_f32 %1, %1, %7, %8\n\tv_max3_f32 %0, %0, %9, %10\n\tv_min3_f32 %1, %1, %9, %10\n\t"
      "v_max3_f32 %0, %0, %11, %12\n\tv_min3_f32 %1, %1, %11, %12\n\tv_max3_f32 %0, %0, %13, %14\n\tv_min3_f32 %1, %1, %13, %14\n\t"
      "v_max3_f32 %0, %0, %15, %16\n\tv_min3_f32 %1, %1, %15, %16\n\tv_max_f32 %0, %0, %17\n\tv_min_f32 %1, %1, %17"
      : "=&v"(mx), "=&v"(mn)
      : "v"(p[0]), "v"(p[1]), "v"(p[2]), "v"(p[3]), "v"(p[4]), "v"(p[5]), "v"(p[6]), "v"(p[7]), "v"(p[8]), "v"(p[9]), "v"(p[10]), "v"(p[11]), "v"(p[12]), "v"(p[13]), "v"(p[14]), "v"(p[15]));
  {
    float mx2, mn2;
    asm("v_mov_b32 %2, %0\n\tv_mov_b32 %3, %1\n\ts_nop 0\n\tv_permlane32_swap_b32 %0, %2\n\tv_permlane32_swap_b32 %1, %3\n\tv_max_f32 %0, %0, %2\n\tv_min_f32 %1, %1, %3"
        : "+v"(mx), "+v"(mn), "=&v"(mx2), "=&v"(mn2));
  }
  const float a = -mx * sc, b = mn * sc;
  UNROLL for (int i = 0; i < 8; ++i) {
    const f32x2 q = f32x2{p[2 * i], p[2 * i + 1]};
    const f32x2 u = __builtin_elementwise_fma(q, f32x2{sc, sc}, f32x2{a, a}), v = __builtin_elementwise_fma(q, f32x2{-sc, -sc}, f32x2{b, b});
    ep[i] = f32x2{__builtin_amdgcn_exp2f(u.x), __builtin_amdgcn_exp2f(u.y)}; en[i] = f32x2{__builtin_amdgcn_exp2f(v.x), __builtin_amdgcn_exp2f(v.y)};
  }
  const f32x2 pa = ((ep[0] + ep[1]) + (ep[2] + ep[3])) + (ep[4] + ep[5]), pb = ep[6] + ep[7];
  const f32x2 na = ((en[0] + en[1]) + (en[2] + en[3])) + (en[4] + en[5]), nb = en[6] + en[7];
  const f32x2 ps = __builtin_elementwise_fma(pb, f32x2{keep, keep}, pa), ns = __builtin_elementwise_fma(nb, f32x2{keep, keep}, na);
  sp = sum32(ps.x + ps.y); sn = sum32(ns.x + ns.y);
}
DI unsigned pk2v(f32x2 v) { return __builtin_bit_cast(unsigned, __builtin_convertvector(v, bf16x2)); }

#define S3SLEEP 3
#define QMID 17
#define B1GR 8
#define B1DIST 14
#define TOUCH0 1
#define TOUCHA 1
#define TOUCHW 1
struct Role {
  unsigned w, xcd, rr, a_p, a_h, par, j0, b_q, b_j, d_t, d_j, rot;
  DI Role(unsigned c0, unsigned w0) {
    const unsigned c = sopq(c0);
    xcd = c & 7; rr = c >> 3; w = sopq(w0);
    a_p = 4 * (xcd >> 1) + (rr >> 3); a_h = 8 * (xcd & 1) + (rr & 7); par = a_h & 1;
    j0 = a_h & 3;
    b_q = 4 * xcd + (rr >> 3); b_j = rr & 7;
    d_t = 8 * xcd + (rr >> 2); d_j = rr & 3;
    rot = B1GR * (d_t & 1);
  }
};

DI void stage_w1(const Params& P, const Role& R, unsigned v, unsigned part, unsigned n, unsigned dst) {
  dma16(mk(P.wqkv, 448 * 1344 * 2), v, n * (16 * 2688) + part * 896 + ((56 * R.a_h) & ~15u), dst + n * 1024);
}
DI void stage_packed(rsrc_t ws, const Role& R, unsigned l, unsigned src, unsigned dst, unsigned N) {
  const unsigned v = l * 16;
  _Pragma("unroll 1") for (unsigned n = R.w ? R.w - 1 : N; n < N; n += 3) {
    const unsigned so = src + n * 4096;
    set_m0(dst + n * 4096); dmac<0>(ws, v, so); dmac<1024>(ws, v, so); dmac<2048>(ws, v, so); dmac<3072>(ws, v, so);
  }
}
DI unsigned wo_voff(unsigned l, int hf) { return (l >> 3) * 896 + 16 * (2 * (((l & 7) >> 1) ^ (((l >> 4) & 1) | (hf << 1))) + (l & 1)); }
DI unsigned a_slot(unsigned w, int j) { return j < 6 ? L_T + w * 12288 + j * 2048 : (j < 9 ? L_WQ + w * 6144 + (j - 6) * 2048 : L_WO + w * 18432 + (j - 9) * 2048); }
template <int N0, int N1, int J0> DI void a_issue(const Params& P, const Role& R, unsigned l) {
  const rsrc_t r0 = mk((R.w >> 1) ? P.ci : P.cr, 2048 * 2048 * 2), r1 = mk((R.w >> 1) ? P.cr : P.ci, 2048 * 2048 * 2);
  const unsigned v0 = (l >> 3) * 4096 + 16 * ((l & 7) ^ (l >> 4)), v1 = (8 + (l >> 3)) * 4096 + 16 * ((l & 7) ^ (4 + (l >> 4))) - 1024;
  const unsigned b0 = (32 * R.d_t + 16 * (R.w & 1)) * 4096 + 1024 * R.w, b1 = b0 ^ 65536;
  UNROLL for (int n = N0; n < N1; ++n) {
    const unsigned so = ((n & 1) ? b1 : b0) + 128 * (((n >> 2) + (R.rot >> 1)) & 7);
    set_m0(a_slot(R.w, J0 + n - N0)); dma<0>((n & 2) ? r1 : r0, v0, so); dma<1024>((n & 2) ? r1 : r0, v1, so);
  }
}
template <int N0, int N1> DI void a_read(const Role& R, unsigned l) {
  const unsigned t = l & 15, f = (t >> 1) & 7, b0 = t * 128 + 16 * ((l >> 4) ^ f), b1 = t * 128 + 16 * ((4 + (l >> 4)) ^ f);
  const unsigned x0 = b0 + a_slot(R.w, 0), x1 = b1 + a_slot(R.w, 0), y0 = b0 + a_slot(R.w, 6), y1 = b1 + a_slot(R.w, 6), z0 = b0 + a_slot(R.w, 9), z1 = b1 + a_slot(R.w, 9);
  UNROLL for (int n = N0; n < N1; ++n) {
    const int j = n - N0, r = 8 * (n >> 2) + (n & 3);
    if (j < 6) { A_LDS_RD(r, x0, j * 2048); A_LDS_RD(r + 4, x1, j * 2048); }
    else if (j < 9) { A_LDS_RD(r, y0, (j - 6) * 2048); A_LDS_RD(r + 4, y1, (j - 6) * 2048); }
    else { A_LDS_RD(r, z0, (j - 9) * 2048); A_LDS_RD(r + 4, z1, (j - 9) * 2048); }
  }
  LGKM0();
}
DI void a_touch(const Params& P, const Role& R, unsigned l, unsigned u0) {
  touch((R.d_j >> 1) ? P.ci : P.cr, (32 * R.d_t + 16 * (R.d_j & 1) + (l >> 2)) * 4096 + 1024 * R.w + 128 * ((u0 + (l & 3) + (R.rot >> 1)) & 7));
}
DI void ws_touch(const Params& P, const Role& R, unsigned i, unsigned pair) {
  const unsigned Pp = R.a_h >> 1;
  const unsigned o1 = WS_XN + pair * 2 * MiB + (R.b_j >> 1) * 458752 + (2 * R.b_q + (R.b_j & 1)) * 7168 + i * 128;
  const unsigned o2 = WS_OF + pair * 2 * MiB + (8 * R.a_p + (i & 7)) * 14336 + (7 * (Pp >> 2) + 3) * 1024 + (Pp & 3) * 256 + R.par * 128;
  const unsigned o3 = WS_G3 + pair * 262144 + R.b_q * 8192 + R.b_j * 1024 + (i & 7) * 128;
  touch(P.ws, i < 56 ? o1 : (i < 64 ? o2 : o3));
}
DI unsigned colsum_all(unsigned l, int part) {
  const unsigned cx = opq(L_CEX + (l & 31) * 4);
  float s = 0.f;
  UNROLL for (int i = 0; i < 4; ++i) s += lds4(cx + (i * 3 + part) * 128);
  return pk2(s, s - lo16(pk2(s, s)));
}
struct A1Pre { v4u wp[2], xa; unsigned pa[8], p0, p1, vb, vw, ko, one; float rstd; };
DI void a1_addresses(A1Pre& A, const Role& R, unsigned l) {
  const unsigned h = l >> 5, tok = l & 31, w = R.w, g = l >> 4, q4 = (l & 15) >> 2;
  const unsigned sz = (tok & 7) | ((((tok >> 1) ^ (tok >> 3)) & 1) << 3), pb = L_MISC + w * 4096 + tok * 128;
  UNROLL for (int G = 0; G < 8; ++G) A.pa[G] = opq(pb + 8 * ((2 * G + h) ^ sz));
  const unsigned U = (w >> 1) * 8 + 4 * (w & 1) + (l & 3), t0 = 16 * (g >> 1) + 4 * (g & 1) + q4, lo3 = 4 * (g & 1) + q4;
  A.p0 = opq(L_MISC + t0 * 128 + 8 * (U ^ (lo3 | (((q4 >> 1) & 1) << 3)))); A.p1 = opq(L_MISC + (t0 + 8) * 128 + 8 * (U ^ (lo3 | ((((q4 >> 1) & 1) ^ 1) << 3))));
  A.vb = opq(L_WO + 12288 + (g >> 1) * 1024 + ((g & 1) * 32 + (l & 15)) * 16); A.vw = opq(L_WO + 12288 + w * 14336 + l * 16);
  A.ko = opq(((g & 1) * 32 + h * 16 + (l & 15)) * 16 + (R.a_h * 16 + R.a_p) * 4096 + w * 1024);
  const unsigned pd = R.par ? 0 : 28;
  A.one = opq(tok == pd ? 0x00003f80u : (tok == pd + 1 ? 0x3f800000u : 0u));
}
DI void a1_wphi(A1Pre& A, const Role& R, unsigned l) {
  const int q4 = (l & 15) >> 2, fq = 4 * ((l >> 4) & 1) + (l & 3);
  UNROLL for (int s = 0; s < 2; ++s) {
    v2u t2[2];
    UNROLL for (int t = 0; t < 2; ++t) {
      const int d0 = 16 * s + 8 * t + 4 * (int)(l >> 5) - 4 * (int)R.par;
      const bool ok = d0 >= 0 && d0 < 28;
      const v2u r = tr8(L_WPHI + ((ok ? d0 : 0) + q4) * 56 + (fq < 7 ? fq : 0) * 8);
      t2[t] = v2u{ok ? r.x : 0u, ok ? r.y : 0u};
    }
    A.wp[s] = cat(t2[0], t2[1]);
  }
  asm volatile("" : "+v"(A.wp[0]), "+v"(A.wp[1]));
}
DI void a1_stats(A1Pre& A, unsigned l, float sum, float ssq) {
  const float mean = sum * (1.f / 448.f), var = fmaxf(ssq * (1.f / 448.f) - mean * mean, 0.f);
  const float sig = __builtin_amdgcn_sqrtf(var + 1e-5f);
  A.rstd = __builtin_amdgcn_rcpf(sig);
  const unsigned mh = pk2(-mean, -mean), ml = pk2(-mean - lo16(mh), -mean - lo16(mh)), sl = pk2(sig, sig - lo16(pk2(sig, sig)));
  A.xa = v4u{l >= 32 ? 0u : mh, l >= 32 ? 0u : ml, l >= 32 ? 0u : sl, 0u};
  asm volatile("" : "+v"(A.xa), "+v"(A.rstd));
}

__global__ void __launch_bounds__(256, 1) fused_fwd(Params P) {
  __shared__ __attribute__((aligned(1024))) char smem[LDS_BYTES];
  asm volatile("" ::"v"((unsigned)(size_t)(LAS char*)smem) : "memory", "a255");
  const char* code; unsigned code_len;
  asm volatile("s_getpc_b64 %0\nLfused_begin:\n\ts_mov_b32 %1, Lfused_end-Lfused_begin" : "=s"(code), "=s"(code_len));
  const unsigned lane = threadIdx.x & 63;
  const unsigned w0 = __builtin_amdgcn_readfirstlane(threadIdx.x >> 6), c0 = blockIdx.x;
  const rsrc_t rWS = mk(P.ws, WS_BYTES);
  STAMP(0); CSTAMP(29);

  v2u res[4];
  v4u xf[4][7];
  f32x16 kT, vv;
  A1Pre A;
  unsigned csk, csv, csq = 0;
  const u64 nonce = *(const __attribute__((address_space(4))) u64*)P.ws;
  auto kv_gemm = [&](unsigned l, unsigned j0, auto&& hook) {
    const unsigned wl = L_T + (56 * (l >> 5) + ((l & 15) >> 2)) * 64 + ((l >> 4) & 1) * 32 + (l & 3) * 8;
    v4u wk[3], wv[3];
    unsigned a;
    auto rd = [&](int s) {
      if (s % 7 == 0) a = opq(wl + ((s / 7) ^ j0) * 7168);
      wk[s % 3] = tr16(a + rhoA(s % 7, 0) * 64, a + rhoA(s % 7, 1) * 64); wv[s % 3] = tr16(a + 28672 + rhoA(s % 7, 0) * 64, a + 28672 + rhoA(s % 7, 1) * 64);
    };
    rd(0); rd(1);
    SB();
    UNROLL for (int s = 0; s < 28; ++s) {
      const v4u x = xf[s / 7][s % 7], k = wk[s % 3], b = wv[s % 3];
      if (s == 0) { MF32z(kT, k, x); } else { MF32(kT, k, x); }
      if (s + 2 < 28) rd(s + 2);
      SB();
      if (s == 0) { MF32z(vv, x, b); } else { MF32(vv, x, b); }
      hook(s, k, b);
      SB();
    }
  };
  auto kv_norm = [&]() { const v4u a1 = v4u{csk, csk, 0u, 0u}, a2 = v4u{csv, csv, A.one, 0u}; nMF32(kT, a1, A.xa); nMF32(vv, A.xa, a2); FENCE2(kT, vv); };

  {
    const Role R(c0, w0);
    const unsigned l = opq(lane) & 63, w = R.w, j0 = R.j0;
    if (TOUCH0) {
      const unsigned i = 64 * w + l, j = i - 112, r12 = (j * 5462) >> 16, k12 = j - 12 * r12;
      const unsigned ox = (R.xcd >> 1) * (512 * 896) + (112 * R.rr + i) * 128;
      const unsigned ow = (14 * R.rr + r12) * 2688 + (k12 >> 2) * 896 + ((R.xcd & 1) * 3 + (k12 & 3)) * 128;
      touch(i < 112 ? P.x : P.wqkv, i < 112 ? ox : ow);
      const unsigned i2 = 16 * w + l, j2 = i2 + 144, r2 = (j2 * 5462) >> 16, k2 = j2 - 12 * r2;
      const unsigned ow2 = (14 * R.rr + r2) * 2688 + (k2 >> 2) * 896 + ((R.xcd & 1) * 3 + (k2 & 3)) * 128, oc = (R.rr * 12 + i2 - 24) * 128;
      if (l < 16 && i2 < 36) touch(i2 < 24 ? P.wqkv : code, i2 < 24 ? ow2 : (oc < code_len ? oc : 0));
    }
    const rsrc_t rX = mk(P.x, 2048 * 448 * 2);
    const unsigned wv0 = (l >> 2) * 2688 + (l & 3) * 16;
    auto w_issue = [&](int Js) { const unsigned n0 = 7 * (j0 ^ Js) + 4 * (w & 1);
      _Pragma("unroll 1") for (unsigned n = n0; n < n0 + 4 - (w & 1); ++n) stage_w1(P, R, wv0, 1 + (w >> 1), n, L_T + (w >> 1) * 28672); };
    auto w_patch = [&](int Js) {
      if (l < 64 - 16 * (w & 1)) sts8(L_T + (w >> 1) * 28672 + (112 * (j0 ^ Js) + 64 * (w & 1) + l) * 64 + (R.par ? 0 : 56), v2u{(w >> 1) ? 0u : 0x00003f80u, 0u}); };
    const unsigned win = w * 16384, xs = (128 * R.a_p + 32 * w) * 896, xc = 16 * ((l & 31) ^ (l >> 5)), xr = (l >> 5) * 896;
    auto x_issue = [&](unsigned hf) { _Pragma("unroll 2") for (unsigned n = 0; n < 16; ++n) dma16(rX, (xc ^ ((32 * n) & 0xf0)) + xr, xs + 384 * hf + n * 1792, win + n * 1024); };
    w_issue(0);
    x_issue(j0 >> 1);
    if ((w & 1) * 64 + l < 98) dma16(mk(P.wphi, 16 * 784 * 2), ((w & 1) * 64 + l) * 16, R.a_h * 1568, L_WPHI + (w & 1) * 1024);
    w_issue(1);
    if (l < 2) {
      const unsigned k = 2 * w + l, kind = k < 2 ? k : (k < 6 ? 2 : k - 3);
      const unsigned id = (kind == 0 || kind == 4) ? R.a_p * 16 + R.a_h : (kind == 2 ? R.d_t * 4 + R.d_j : R.b_q * 8 + R.b_j);
      asm volatile("buffer_atomic_add %0, %1, %2, 0 offen" ::"v"(0u), "v"(WS_MB + kind * 131072 + id * 512 + (kind == 2 ? (k - 2) * 128 : 0)), "s"(rWS) : "memory");
    }
    a1_addresses(A, R, l);
    const unsigned xb = opq(win + (l & 31) * 512), sw = (l & 15) << 4;
    auto x_read = [&](int Js, int i) {
      const unsigned J = j0 ^ Js, o = 224 * J - 384 * (J >> 1) + 112 * (l >> 5);
      xf[Js][i] = cat(lds8(xb + ((o + 2 * rhoA(i, 0)) ^ sw)), lds8(xb + ((o + 2 * rhoA(i, 1)) ^ sw)));
    };
    float q0 = 0.f, q1 = 0.f, q2 = 0.f, q3 = 0.f;
    auto sq = [&](int Js) { UNROLL for (int i = 0; i < 7; ++i) { const v4u x = xf[Js][i]; q0 = dot2(x.x, x.x, q0); q1 = dot2(x.y, x.y, q1); q2 = dot2(x.z, x.z, q2); q3 = dot2(x.w, x.w, q3); }
      asm volatile("" : "+v"(q0), "+v"(q1), "+v"(q2), "+v"(q3)); };
    const unsigned o1 = (l & 15) == ((l >> 4) & 1) ? 0x3f803f80u : 0u;
    v4u ones = v4u{o1, o1, o1, o1};
    f32x4 ck = {}, cv = {};
    asm volatile("" : "+v"(ck), "+v"(cv), "+v"(ones));
    STAMP(31);
    VMC(4);
    STAMP(32);
    w_patch(0);
    UNROLL for (int i = 0; i < 14; ++i) x_read(i / 7, i % 7);
    BAR();
    w_issue(2);
    x_issue((j0 >> 1) ^ 1);
    w_issue(3);
    STAMP(1);
    const unsigned ro = (64 * R.b_q + 16 * w + (l & 15)) * 896 + (56 * R.b_j + 4 * (l >> 4)) * 2;
    v4u pk0, pk1;
    kv_gemm(l, j0, [&](int s, const v4u& k, const v4u& b) {
      asm volatile("s_cmp_lg_u32 %5, %6\n\ts_cbranch_scc1 Lc%=\n\tv_mfma_f32_16x16x32_bf16 %0, %2, %3, %0\n\tv_mfma_f32_16x16x32_bf16 %1, %2, %4, %1\n\tLc%=:"
                   : "+v"(ck), "+v"(cv) : "v"(ones), "v"(k), "v"(b), "s"(w), "i"(s & 3) : "scc");
      if (s == 1) a1_wphi(A, R, l);
      if (s == 4) { VMC(22); w_patch(1); BAR(); }
      if (s == 8) { sq(0); sq(1); VMC(3); w_patch(2); BAR(); }
      if (s >= 8 && s < 13) { UNROLL for (int i = 3 * (s - 8); i < 3 * (s - 8) + 3 && i < 14; ++i) x_read(2 + i / 7, i % 7); }
      if (s >= 14 && s < 18) asm volatile("buffer_load_dwordx2 %0, %1, %2, 0 offen" : "=&v"(res[s - 14]) : "v"((s == 17 && l >= 32) ? OOB : ro + 32 * (s - 14)), "s"(rX) : "memory");
      if (s == 18) { VMC(4); w_patch(3); BAR(); }
      if (s >= 19 && s < 26) stage_w1(P, R, wv0, 0, 7 * w + s - 19, L_WQ);
      if (s == 19) { const unsigned a = L_T + (w & 1) * 28672 + R.a_p * 1792 + l * 16; pk0 = lds16(a); pk1 = lds16(a + 1024); }
      if (s == 26) {
        gst16<SC1>((w >> 1) ? pk1 : pk0, rWS, (w >> 1) && l >= 48 ? OOB : l * 16, WS_WPK + R.a_h * 86016 + (w & 1) * 28672 + R.a_p * 1792 + (w >> 1) * 1024);
        if (TOUCHW) { if (w < 2) { if (l < 49) touch(P.wo, (98 * R.rr + 49 * w + l) * 128); } else if (l < 36) ws_touch(P, R, 36 * (w - 2) + l, 0); }
      }
      if (s == 27 && TOUCHA) a_touch(P, R, l, 0);
    });
    sq(2); sq(3);
    asm volatile("s_nop 3" : "+v"(ck), "+v"(cv), "+v"(kT), "+v"(vv));
    if (l < 16) { const unsigned cx = L_CEX + w * 384 + l * 4; sts4(cx, ck[0]); sts4(cx + 64, ck[1]); sts4(cx + 128, cv[0]); sts4(cx + 192, cv[1]); }
    {
      const float sx = R.par ? kT[0] : kT[12];
      auto r = __builtin_amdgcn_permlane32_swap(asu(sx), asu(sx), false, false);
      a1_stats(A, l, asf(R.par ? r[0] : r[1]), sum32((q0 + q1) + (q2 + q3)));
    }
    BAR();
    csk = colsum_all(l, 0); csv = colsum_all(l, 1);
    kv_norm();
    asm volatile("s_waitcnt vmcnt(%c4)" : "+v"(res[0]), "+v"(res[1]), "+v"(res[2]), "+v"(res[3]) : "i"(1 + TOUCHW + TOUCHA) : "memory");
  }

  _Pragma("unroll 1") for (unsigned pair = 0; pair < 2; ++pair) {
    const u64 tag0 = nonce + 1 + 6 * pair;
    {
      STAMP(2 + 14 * pair);
      const float sc = A.rstd * 1.44269504088896f, keep = lane >= 32 ? 0.f : 1.f;
      {
        f32x16 pk;
        const v4u k0 = pack8(kT, 0), k1 = pack8(kT, 1);
        nMF32z(pk, A.wp[0], k0); nMF32(pk, A.wp[1], k1);
        sts16(A.vw, pack8(vv, 0)); sts16(A.vw + 1024, pack8(vv, 1));
        FENCE1(pk);
        f32x2 ep[8], en[8]; float sp, sn;
        dual_exp(pk, keep, sc, ep, en, sp, sn);
        const float fp = A.rstd * __builtin_amdgcn_rcpf(sp), fn = A.rstd * __builtin_amdgcn_rcpf(sn);
        UNROLL for (int G = 0; G < 4; ++G) {
          sts8(A.pa[G], v2u{pk2v(ep[2 * G] * fp), pk2v(ep[2 * G + 1] * fp)});
          sts8(A.pa[4 + G], v2u{pk2v(en[2 * G] * fn), pk2v(en[2 * G + 1] * fn)});
        }
      }
      BAR();
      {
        v4u a[4], b0[4], b1[4];
        UNROLL for (int i = 0; i < 4; ++i) { a[i] = tr16(A.p0 + i * 4096, A.p1 + i * 4096); b0[i] = lds16(A.vb + i * 14336); b1[i] = lds16(A.vb + i * 14336 + 256); }
        f32x4 d0, d1;
        MF16z(d0, a[0], b0[0]); MF16z(d1, a[0], b1[0]);
        UNROLL for (int i = 1; i < 4; ++i) { MF16(d0, a[i], b0[i]); MF16(d1, a[i], b1[i]); }
        asm volatile("s_nop 7" : "+v"(d0), "+v"(d1));
        auto r0 = __builtin_amdgcn_permlane32_swap(pk2(d0[0], d0[1]), pk2(d1[0], d1[1]), false, false);
        auto r1 = __builtin_amdgcn_permlane32_swap(pk2(d0[2], d0[3]), pk2(d1[2], d1[3]), false, false);
        gst16<SC1>(v4u{r0[0], r1[0], r0[1], r1[1]}, rWS, A.ko, WS_KVP + pair * MiB);
      }
      STAMP(3 + 14 * pair);
      const Role R(c0, w0);
      const unsigned l = opq(lane) & 63, h = l >> 5, w = R.w, par = R.par;
      v4u phq[4]; float spq;
      const unsigned mb1 = (w == 0 && l < 16) ? WS_MB + (R.a_p * 16 + R.a_h) * 512 + l * 8 : OOB;
      u64 seen;
      {
        f32x16 qT;
        v4u wq[5];
        unsigned a;
        const unsigned wl = L_WQ + (56 * h + ((l & 15) >> 2)) * 64 + ((l >> 4) & 1) * 32 + (l & 3) * 8;
        auto rd = [&](int s) { if (s % 7 == 0) a = opq(wl + ((s / 7) ^ R.j0) * 7168); wq[s % 5] = tr16(a + rhoA(s % 7, 0) * 64, a + rhoA(s % 7, 1) * 64); };
        const rsrc_t rWO = mk(P.wo, 448 * 448 * 2);
        const unsigned ov0 = wo_voff(l, 0), ov1 = wo_voff(l, 1);
        rd(0); rd(1); rd(2); rd(3);
        SB();
        UNROLL for (int s = 0; s < 28; ++s) {
          if (s == 0) { MF32z(qT, wq[0], xf[0][0]); } else { MF32(qT, wq[s % 5], xf[s / 7][s % 7]); }
          if (s + 4 < 28) rd(s + 4);
          if (s & 1) dma16(rWO, s < 14 ? ov0 : ov1, (14 * w + (s >> 1)) * 8 * 896 + 112 * R.b_j, L_WO + (14 * w + (s >> 1)) * 1024);
          if (s == QMID) {
            VMC((QMID + 1) / 2);
            asm volatile("s_barrier" ::: "memory");
            if (w == 1 && l < 16) post(rWS, WS_MB + (l * 16 + R.a_h) * 512 + R.a_p * 8, tag0);
          }
          SB();
        }
        if (pair == 0) {
          if (TOUCHA) a_touch(P, R, l, 4);
          a_issue<0, 5, 0>(P, R, l);
          v4u ones = v4u{0x3f803f80u, 0x3f803f80u, 0x3f803f80u, 0x3f803f80u};
          asm volatile("" : "+v"(ones));
          const unsigned aq = opq(wl + w * 7168);
          f32x16 cq;
          UNROLL for (int i = 0; i < 7; ++i) { const v4u t = tr16(aq + i * 512, aq + i * 512 + 256); if (i == 0) { nMF32z(cq, ones, t); } else { MF32(cq, ones, t); } }
          if (w < 2) gst16<SC1>(lds16(L_WQ + R.a_p * 1792 + w * 1024 + l * 16), rWS, w && l >= 48 ? OOB : l * 16, WS_WPK + R.a_h * 86016 + 57344 + R.a_p * 1792 + w * 1024);
          FENCE1(cq);
          if (h == 0) sts4(L_CEX + (w * 3 + 2) * 128 + l * 4, cq[0]);
          BAR();
          csq = colsum_all(l, 2);
        }
        const v4u a0 = v4u{csq, csq, 0u, 0u};
        nMF32(qT, a0, A.xa);
        FENCE1(qT);
        f32x16 pq;
        const v4u q0 = pack8(qT, 0), q1 = pack8(qT, 1);
        nMF32z(pq, A.wp[0], q0); nMF32(pq, A.wp[1], q1);
        FENCE1(pq);
        seen = poll_start(rWS, mb1);
        SB();
        f32x2 ep[8], en[8]; float sn;
        dual_exp(pq, keep, sc, ep, en, spq, sn);
        const float fn = spq * __builtin_amdgcn_rcpf(sn), fk = fn * keep;
        phq[0] = v4u{pk2v(ep[0]), pk2v(ep[1]), pk2v(ep[2]), pk2v(ep[3])};
        phq[1] = v4u{pk2v(ep[4]), pk2v(ep[5]), pk2v(ep[6] * keep), pk2v(ep[7] * keep)};
        phq[2] = v4u{pk2v(en[0] * fn), pk2v(en[1] * fn), pk2v(en[2] * fn), pk2v(en[3] * fn)};
        phq[3] = v4u{pk2v(en[4] * fn), pk2v(en[5] * fn), pk2v(en[6] * fk), pk2v(en[7] * fk)};
        asm volatile("" : "+v"(phq[0]), "+v"(phq[1]), "+v"(phq[2]), "+v"(phq[3]), "+v"(spq));
      }
      v4u sel;
      UNROLL for (int e = 0; e < 4; ++e) {
        const unsigned r0 = ((2 * e) & 3) + 8 * ((2 * e) >> 2) + 4 * h;
        sel[e] = opq((l & 31) == r0 ? 0x00003f80u : ((l & 31) == r0 + 1 ? 0x3f800000u : 0u));
      }
      const unsigned Pp = R.a_h >> 1, l16 = opq(l * 16), kb = opq(L_MISC + l * 16);
      const unsigned so = sopq(WS_OF + pair * 2 * MiB + (8 * R.a_p + 2 * w) * 14336 + 7 * (Pp >> 2) * 1024 + (Pp & 3) * 256);
      const unsigned gs = sopq(WS_KVP + pair * MiB + R.a_h * 65536 + w * 1024);
      const unsigned vo = ((l >> 4) & 1) * 14336 + (l & 15) * 16;
      const bool full2 = h == par, useB = par && !h;
      const unsigned vo1 = opq(vo + (par ? (h ? 4 : 6) : (h ? 2 : 0)) * 1024), vo2 = opq(full2 ? vo + (par ? 5 : 1) * 1024 : OOB), vo3 = opq(full2 ? OOB : vo + 3 * 1024 + 8 * par);
      STAMP(4 + 14 * pair);
      asm volatile("s_waitcnt vmcnt(0)" : "+v"(seen)::"memory");
      if (w == 0 && l < 16) poll_end(rWS, mb1, tag0, seen);
      BAR();
      STAMP(5 + 14 * pair);
      v4u pt[16];
      UNROLL for (int i = 0; i < 16; ++i) { pt[i] = gld16<SC1>(rWS, l16, gs + i * 4096); SB(); }
      {
        f32x16 t;
        MF32z(t, sel, pt[0]);
        UNROLL for (int i = 1; i < 16; ++i) MF32(t, sel, pt[i]);
        FENCE1(t);
        sts16(kb + w * 1024, pack8(t, 0));
      }
      BAR();
      f32x16 n;
      v4u kf[4];
      UNROLL for (int i = 0; i < 4; ++i) kf[i] = lds16(kb + i * 1024);
      MF32z(n, kf[0], phq[0]); MF32(n, kf[1], phq[1]); MF32(n, kf[2], phq[2]); MF32(n, kf[3], phq[3]);
      FENCE1(n);
      const float ds = par ? n[0] + n[1] : n[12] + n[13];
      auto r = __builtin_amdgcn_permlane32_swap(asu(ds), asu(ds), false, false);
      const float inv = __builtin_amdgcn_rcpf(asf(par ? r[0] : r[1]) + 1e-6f * spq);
      v2u o[4];
      UNROLL for (int G = 0; G < 4; ++G) o[G] = v2u{pk2(n[4 * G] * inv, n[4 * G + 1] * inv), pk2(n[4 * G + 2] * inv, n[4 * G + 3] * inv)};
      gst16<SC1>(useB ? cat(o[2], o[3]) : cat(o[0], o[1]), rWS, vo1, so);
      gst16<SC1>(cat(o[2], o[3]), rWS, vo2, so);
      gst8<SC1>(par ? o[1] : o[2], rWS, vo3, so);
      STAMP(6 + 14 * pair);
      if (pair == 0) { a_read<0, 5>(R, l); a_issue<5, 10, 0>(P, R, l); VMC(10); } else DRAIN();
      BAR();
      if (w == 1 && l < 16) post(rWS, WS_MB + 131072 + ((2 * R.a_p + (l >> 3)) * 8 + (l & 7)) * 512 + R.a_h * 8, tag0 + 1);
      if (pair == 0) a_issue<10, 14, 5>(P, R, l);
    }
    {
      const Role R(c0, w0);
      const unsigned l = opq(lane) & 63, g = l >> 4, q4 = (l & 15) >> 2, w = R.w;
      f32x4 acc[4];
      {
        constexpr int PRE = 8, LEAD = 12;
        const unsigned f = ((q4 >> 1) & 1) | ((g & 1) << 1), wb = L_WO + (56 * g + q4) * 128 + (l & 3) * 8;
        unsigned ba[4];
        UNROLL for (int a = 0; a < 4; ++a) ba[a] = opq(wb + 32 * (a ^ f));
        v4u wf[PRE][4];
        auto rd = [&](int s, int a) { wf[s % PRE][a] = tr16(ba[a] + (224 * (s / 7) + rhoO(s % 7, 0)) * 128, ba[a] + (224 * (s / 7) + rhoO(s % 7, 1)) * 128); };
        UNROLL for (int s = 0; s < PRE; ++s) UNROLL for (int a = 0; a < 4; ++a) rd(s, a);
        UNROLL for (int a = 0; a < 4; ++a) acc[a] = f32x4{lo16(res[a].x), hi16(res[a].x), lo16(res[a].y), hi16(res[a].y)};
        asm volatile("" : "+v"(acc[0]), "+v"(acc[1]), "+v"(acc[2]), "+v"(acc[3]));
        const unsigned l16 = opq(l * 16) & 1023, os = sopq(WS_OF + pair * 2 * MiB + (4 * R.b_q + w) * 14336);
        if (w == 0 && l < 16) poll(rWS, WS_MB + 131072 + (R.b_q * 8 + R.b_j) * 512 + l * 8, tag0 + 1);
        BAR();
        STAMP(7 + 14 * pair);
        v4u of[14];
        auto ld = [&](int s) { of[s] = gld16<SC1>(rWS, l16 + (s & 3) * 1024, os + (s >> 2) * 4096); };
        UNROLL for (int s = 0; s < LEAD; ++s) { ld(s); SB(); }
        UNROLL for (int s = 0; s < 14; ++s)
          UNROLL for (int a = 0; a < 4; ++a) { MF16(acc[a], wf[s % PRE][a], of[s]); if (a == 0 && s + LEAD < 14) ld(s + LEAD); if (s + PRE < 14) rd(s + PRE, a); SB(); }
        FENCE4(acc);
      }
      UNROLL for (int e = 0; e < 4; ++e) acc[3][e] = l >= 32 ? 0.f : acc[3][e];
      const f32x4 t4 = (acc[0] + acc[1]) + (acc[2] + acc[3]), u4 = (acc[0] * acc[0] + acc[1] * acc[1]) + (acc[2] * acc[2] + acc[3] * acc[3]);
      const float s1 = sum32(sum16((t4[0] + t4[1]) + (t4[2] + t4[3]))), s2 = sum32(sum16((u4[0] + u4[1]) + (u4[2] + u4[3])));
      const unsigned tg = (unsigned)tag0 + 2;
      const unsigned g3 = WS_G3 + pair * 262144 + R.b_q * 8192 + w * 256;
      gst16<SC1>(v4u{asu(s1), tg, asu(s2), tg}, rWS, l < 16 ? l * 16 : OOB, g3 + R.b_j * 1024);
      STAMP(8 + 14 * pair);
      UNROLL for (int a = 0; a < 4; ++a) res[a] = v2u{pk2(acc[a][0], acc[a][1]), pk2(acc[a][2], acc[a][3])};
      const unsigned img = L_TOP + w * 2048;
      unsigned iw[4], ir[2][2], xo[2];
      UNROLL for (int a = 0; a < 4; ++a) iw[a] = opq(img + (l & 15) * 128 + 8 * ((4 * a + g) ^ (l & 15)));
      UNROLL for (int n = 0; n < 2; ++n) {
        const unsigned T = 4 * n + g, o = T & 1, QS = T >> 1, t0 = 8 * o + q4;
        ir[n][0] = opq(img + t0 * 128 + 8 * ((4 * QS + (l & 3)) ^ t0)); ir[n][1] = opq(img + (t0 + 4) * 128 + 8 * ((4 * QS + (l & 3)) ^ (t0 + 4)));
        const unsigned quad = 4 * QS + q4, cq = 56 * (R.b_j & 1) + 4 * quad;
        const unsigned tile = cq < 96 ? 2 * (cq >> 5) + ((cq >> 2) & 1) : 6, ii = (cq < 96 ? 4 * ((cq & 31) >> 3) : cq - 96) + (l & 3);
        xo[n] = opq(quad < 14 ? tile * 1024 + ((2 * (w & 1) + o) * 16 + ii) * 16 : OOB);
      }
      const unsigned xs = sopq(WS_XN + pair * 2 * MiB + (R.b_j >> 1) * 458752 + (2 * R.b_q + (w >> 1)) * 7168), gv = opq((l & 15) * 16 + g * 2048) & 0xffff;
      if (pair == 0) {
        BAR();
        a_read<5, 14>(R, l);
        a_issue<14, 18, 0>(P, R, l);
        if (TOUCHW && l < 49) touch((w >> 1) ? P.hi : P.hr, (98 * R.rr + 49 * (w & 1) + l) * 128);
      } else __builtin_amdgcn_s_sleep(S3SLEEP);
      v4u ga, gb;
      _Pragma("unroll 1") for (unsigned spin = 0; spin < 0x10000; ++spin) {
        ga = gld16<SC1>(rWS, gv, g3); gb = gld16<SC1>(rWS, gv + 1024, g3);
        if (__all(ga.y == tg && ga.w == tg && gb.y == tg && gb.w == tg)) break;
        asm volatile("" ::: "memory");
      }
      STAMP(9 + 14 * pair);
      const float t1 = sum32(sum16(asf(ga.x) + asf(gb.x))), t2 = sum32(sum16(asf(ga.z) + asf(gb.z)));
      const float mean = t1 * (1.f / 448.f), var = fmaxf(t2 * (1.f / 448.f) - mean * mean, 0.f);
      const float rstd = __builtin_amdgcn_rsqf(var + 1e-5f), nb = -mean * rstd;
      UNROLL for (int a = 0; a < 4; ++a) { const f32x4 y = acc[a] * rstd + nb; sts8(iw[a], v2u{pk2(y[0], y[1]), pk2(y[2], y[3])}); }
      UNROLL for (int n = 0; n < 2; ++n) gst16<SC1>(tr16(ir[n][0], ir[n][1]), rWS, xo[n], xs);
      if (pair == 0) { a_issue<18, 23, 4>(P, R, l); VMC(10); } else DRAIN();
      BAR();
      if (w == 1) post(rWS, WS_MB + 2 * 131072 + (l * 4 + (R.b_j >> 1)) * 512 + (2 * R.b_q + (R.b_j & 1)) * 8, tag0 + 3);
      if (pair == 0) { a_issue<23, 32, 9>(P, R, l); DRAIN(); a_read<14, 32>(R, l); }
    }
    {
      const Role R(c0, w0);
      const unsigned l = opq(lane) & 63, w = R.w;
      constexpr int NB = B1DIST / 7 + 1 + (B1DIST % 7 != 0), HS = 112 - B1DIST;
      f32x4 acc[4][7];
      v4u b[NB][7];
      const unsigned l16 = opq(l * 16), xb = sopq(WS_XN + pair * 2 * MiB + R.d_j * 458752 + 16 * w * 7168), rot = sopq(R.rot);
      unsigned hv[4];
      UNROLL for (int o = 0; o < 4; ++o) hv[o] = opq((l >> 3) * 896 + 16 * ((l & 7) ^ (4 * (o & 1) + (l >> 4))) + o * 6144);
      if (w == 0) poll(rWS, WS_MB + 2 * 131072 + (R.d_t * 4 + R.d_j) * 512 + l * 8, tag0 + 3);
      BAR();
      STAMP(10 + 14 * pair);
      auto ldb = [&](auto I) {
        constexpr int s = I.v / 7, n = I.v % 7;
        b_load<(n & 3) * 1024>(b[s % NB][n], rWS, l16, xb + ((s + rot) & 15) * 7168 + (n >> 2) * 4096);
      };
      auto h_dma = [&](auto I) {
        constexpr int k = I.v / 7, o = I.v % 7;
        const unsigned n = w + 4 * k, m = n >= 7;
        const rsrc_t rH = mk(m ? P.hi : P.hr, 448 * 448 * 2);
        const unsigned so = (56 * R.b_j + (o < 4 ? 0 : 32)) * 896 + (n - 7 * m) * 128;
        if (k < 3 || w < 2) {
          if (o == 0 || o == 4) set_m0(n * 7168 + o * 1024);
          dma<(o & 3) * 1024>(rH, hv[o & 3], so);
        }
      };
      static_for<0, B1DIST>(ldb);
      static_for<0, 112>([&](auto I) {
        constexpr int i = I.v, s = i / 7, n = i % 7, hd = i > HS ? 2 * (i - HS) : 0;
        b_wait<(i + B1DIST < 112 ? B1DIST - 1 : 111 - i) + (hd < 21 ? hd : 21)>(b[s % NB][n]);
        static_for<0, 4>([&](auto M) {
          constexpr int m = M.v;
          mf16a<4 * s + m, s == 0>(acc[m][n], b[s % NB][n]);
          if constexpr (m == 0 && i < HS) ldb(IC<i + B1DIST>{});
          if constexpr (i >= HS && (m == 0 || m == 2) && 2 * (i - HS) + m / 2 < 28) h_dma(IC<2 * (i - HS) + m / 2>{});
        });
        SB();
      });
      asm volatile("s_nop 7");
      PIN7(acc[0]); PIN7(acc[1]); PIN7(acc[2]); PIN7(acc[3]);
      STAMP(11 + 14 * pair);
      const unsigned r1 = opq(L_T + w * 14336 + l * 16), r2 = opq(L_T + (w ^ 1) * 14336 + l * 16), r3 = opq(L_T + (w ^ 3) * 14336 + l * 16);
      UNROLL for (int n = 0; n < 7; ++n) sts16(r1 + (7 + n) * 1024, __builtin_bit_cast(v4u, acc[3][n]));
      UNROLL for (int n = 0; n < 7; ++n) sts16(r1 + n * 1024, __builtin_bit_cast(v4u, acc[1][n]));
      BAR();
      UNROLL for (int n = 0; n < 7; ++n) acc[2][n] += __builtin_bit_cast(f32x4, lds16(r2 + (7 + n) * 1024));
      UNROLL for (int n = 0; n < 7; ++n) sts16(r2 + (7 + n) * 1024, __builtin_bit_cast(v4u, acc[2][n]));
      acc[0][6] += __builtin_bit_cast(f32x4, lds16(r2 + 6 * 1024));
      sts16(r2 + 6 * 1024, __builtin_bit_cast(v4u, acc[0][6]));
      UNROLL for (int n = 0; n < 6; ++n) acc[0][n] += __builtin_bit_cast(f32x4, lds16(r2 + n * 1024));
      BAR();
      UNROLL for (int n = 0; n < 7; ++n) acc[0][n] += __builtin_bit_cast(f32x4, lds16(r3 + (7 + n) * 1024));
      const f32x4 other = acc[2][6] + __builtin_bit_cast(f32x4, lds16(r3 + 6 * 1024));
      const unsigned sg = (w >> 1) ? 0x80008000u : 0u;
      v2u o[7];
      UNROLL for (int n = 0; n < 7; ++n) o[n] = v2u{pk2(acc[0][n][0], acc[0][n][1]) ^ sg, pk2(acc[0][n][2], acc[0][n][3]) ^ sg};
      const unsigned so = WS_PQ + pair * 4 * MiB + (2 * R.d_t + (w & 1)) * 28672 + 7 * R.d_j * 1024;
      UNROLL for (int u = 0; u < 3; ++u) gst16<SC1>(cat(o[2 * u], o[2 * u + 1]), rWS, l * 16 + u * 1024, so + (w >> 1) * 3072);
      if (w < 2) gst16<SC1>(cat(o[6], v2u{pk2(other[0], other[1]) ^ 0x80008000u, pk2(other[2], other[3]) ^ 0x80008000u}), rWS, l * 16, so + 6144);
      STAMP(12 + 14 * pair);
      DRAIN();
      BAR();
      if (w == 1 && l < 8) post(rWS, WS_MB + 3 * 131072 + ((R.d_t >> 1) * 8 + l) * 512 + (4 * (R.d_t & 1) + R.d_j) * 8, tag0 + 4);
      if (pair == 0) {
        if (TOUCHW && w >= 2 && l < 36) ws_touch(P, R, 36 * (w - 2) + l, 1);
        stage_packed(rWS, R, l, WS_WPK + R.a_h * 86016, L_T, 14);
      }
    }
    {
      const Role R(c0, w0);
      const unsigned l = opq(lane) & 63, g = l >> 4, e16 = l & 15, f = (e16 >> 1) & 7, w = R.w;
      f32x4 acc[4];
      {
        constexpr int PRE = 7, LEAD = 12;
        unsigned hb[2][4], hm[2][4];
        UNROLL for (int k = 0; k < 4; ++k) {
          hb[0][k] = opq(((2 * k + g) >> 3) * 7168 + e16 * 128 + 16 * (((2 * k + g) & 7) ^ f)); hb[1][k] = opq(hb[0][k] + L_HI);
          hm[0][k] = opq(e16 * 128 + 16 * ((2 * k + (g >> 1)) ^ f) + 8 * (g & 1)); hm[1][k] = opq(hm[0][k] + L_HI);
        }
        v4u wf[PRE][4];
        auto rd = [&](int s, int a) {
          const int j = s / 7, i = s % 7, c = 14 * j + (i < 6 ? 4 * (i % 3) : 12);
          if (i < 6) wf[s % PRE][a] = lds16(hb[i / 3][(c & 7) >> 1] + (c >> 3) * 7168 + a * 2048);
          else wf[s % PRE][a] = cat(lds8(hm[0][(c & 7) >> 1] + (c >> 3) * 7168 + a * 2048), lds8(hm[1][(c & 7) >> 1] + (c >> 3) * 7168 + a * 2048));
        };
        UNROLL for (int s = 0; s < PRE; ++s) { UNROLL for (int a = 0; a < 4; ++a) { rd(s, a); asm volatile("" ::: "memory"); } }
        UNROLL for (int a = 0; a < 4; ++a) acc[a] = f32x4{lo16(res[a].x), hi16(res[a].x), lo16(res[a].y), hi16(res[a].y)};
        asm volatile("" : "+v"(acc[0]), "+v"(acc[1]), "+v"(acc[2]), "+v"(acc[3]));
        const unsigned l16 = opq(l * 16) & 1023, ps = sopq(WS_PQ + pair * 4 * MiB + (4 * R.b_q + w) * 28672);
        if (w == 0 && l < 8) poll(rWS, WS_MB + 3 * 131072 + (R.b_q * 8 + R.b_j) * 512 + l * 8, tag0 + 4);
        BAR();
        STAMP(13 + 14 * pair);
        v4u pf[28];
        auto ld = [&](int s) { pf[s] = gld16<SC1>(rWS, l16 + (s & 3) * 1024, ps + (s >> 2) * 4096); };
        UNROLL for (int s = 0; s < LEAD; ++s) { ld(s); SB(); }
        UNROLL for (int s = 0; s < 28; ++s)
          UNROLL for (int a = 0; a < 4; ++a) { MF16(acc[a], wf[s % PRE][a], pf[s]); if (a == 0 && s + LEAD < 28) ld(s + LEAD); if (s + PRE < 28) rd(s + PRE, a); SB(); }
        FENCE4(acc);
      }
      UNROLL for (int a = 0; a < 4; ++a) res[a] = v2u{pk2(acc[a][0], acc[a][1]), pk2(acc[a][2], acc[a][3])};
      if (pair == 1) {
        const rsrc_t rO = mk(P.out, 2048 * 448 * 2);
        const unsigned ro = (64 * R.b_q + 16 * w + e16) * 896 + (56 * R.b_j + 4 * g) * 2;
        UNROLL for (int a = 0; a < 4; ++a) gst8<0>(res[a], rO, (a == 3 && l >= 32) ? OOB : ro + 32 * a, 0);
        UNROLL for (int Js = 0; Js < 4; ++Js)
          asm volatile("" : "=v"(xf[Js][0]), "=v"(xf[Js][1]), "=v"(xf[Js][2]), "=v"(xf[Js][3]), "=v"(xf[Js][4]), "=v"(xf[Js][5]), "=v"(xf[Js][6]));
        asm volatile("" : "=v"(kT), "=v"(vv), "=v"(A.wp[0]), "=v"(A.wp[1]), "=v"(A.xa), "=v"(A.rstd), "=v"(A.p0), "=v"(A.p1), "=v"(A.vb), "=v"(A.vw), "=v"(A.ko), "=v"(A.one));
        asm volatile("" : "=v"(A.pa[0]), "=v"(A.pa[1]), "=v"(A.pa[2]), "=v"(A.pa[3]), "=v"(A.pa[4]), "=v"(A.pa[5]), "=v"(A.pa[6]), "=v"(A.pa[7]));
      } else {
        const unsigned so = WS_X2 + (2 * R.b_q + (w >> 1)) * 28672 + 7 * (R.b_j >> 1) * 1024 + (R.b_j & 1) * 512 + (w & 1) * 256;
        gst16<SC1>(cat(res[0], res[1]), rWS, g * 1024 + e16 * 16, so);
        gst16<SC1>(cat(res[2], res[3]), rWS, l < 32 ? (4 + g) * 1024 + e16 * 16 : OOB, so);
        gst8<SC1>(res[2], rWS, l < 32 ? OOB : 6 * 1024 + e16 * 16 + (g & 1) * 8, so);
        UNROLL for (int e = 0; e < 4; ++e) acc[3][e] = l >= 32 ? 0.f : acc[3][e];
        const f32x4 t4 = (acc[0] + acc[1]) + (acc[2] + acc[3]), u4 = (acc[0] * acc[0] + acc[1] * acc[1]) + (acc[2] * acc[2] + acc[3] * acc[3]);
        const float s1 = sum32(sum16((t4[0] + t4[1]) + (t4[2] + t4[3]))), s2 = sum32(sum16((u4[0] + u4[1]) + (u4[2] + u4[3])));
        gst8<SC1>(v2u{asu(s1), asu(s2)}, rWS, l < 16 ? l * 8 : OOB, WS_SSQ + (4 * R.b_q + w) * 1024 + R.b_j * 128);
        STAMP(14);
        DRAIN();
        BAR();
        if (w == 1 && l < 16) post(rWS, WS_MB + 4 * 131072 + ((R.b_q >> 1) * 16 + l) * 512 + (8 * (R.b_q & 1) + R.b_j) * 8, tag0 + 5);
        stage_packed(rWS, R, l, WS_WPK + R.a_h * 86016 + 57344, L_WQ, 7);
        a1_addresses(A, R, l);
        a1_wphi(A, R, l);
        const unsigned l16 = opq(l * 16) & 1023, sv = opq(((l >> 4) & 1) * 1024 + (l >> 5) * 512 + e16 * 8) & 0xffff;
        const unsigned xs = sopq(WS_X2 + (4 * R.a_p + w) * 28672), ss = sopq(WS_SSQ + (8 * R.a_p + 2 * w) * 1024), j0 = sopq(R.j0);
        if (w == 0 && l < 16) poll(rWS, WS_MB + 4 * 131072 + (R.a_p * 16 + R.a_h) * 512 + l * 8, tag0 + 5);
        BAR();
        STAMP(15);
        constexpr int LEAD = 11;
        auto ld = [&](int s) { xf[s / 7][s % 7] = gld16<SC1>(rWS, l16 + ((s % 7) & 3) * 1024, xs + ((s / 7) ^ j0) * 7168 + ((s % 7) >> 2) * 4096); };
        UNROLL for (int s = 0; s < LEAD; ++s) { ld(s); SB(); }
        v2u st[4];
        kv_gemm(l, j0, [&](int s, const v4u&, const v4u&) {
          if (s + LEAD < 28) ld(s + LEAD);
          else if (s + LEAD < 32) st[s + LEAD - 28] = gld8<SC1>(rWS, sv + (s + LEAD - 28) * 128, ss);
          if (s == 27) a1_stats(A, l, sum32((asf(st[0].x) + asf(st[1].x)) + (asf(st[2].x) + asf(st[3].x))), sum32((asf(st[0].y) + asf(st[1].y)) + (asf(st[2].y) + asf(st[3].y))));
        });
        kv_norm();
      }
    }
  }
  if (c0 == 0 && w0 == 0 && lane == 0) {
    const u64 nn = nonce + 16;
    gst8<SC1>(v2u{(unsigned)nn, (unsigned)(nn >> 32)}, rWS, 0, WS_NONCE);
  }
  asm volatile("Lfused_end:");
}

}

extern "C" void kernel_launch(void* const* d_in, const int* in_sizes, int n_in, void* d_out, int out_size, void* d_ws, size_t ws_size,
                              hipStream_t stream) {
  fused::Params p;
  p.x = d_in[0]; p.wqkv = d_in[1]; p.wphi = d_in[2]; p.wo = d_in[3];
  p.cr = d_in[5]; p.ci = d_in[6]; p.hr = d_in[7]; p.hi = d_in[8];
  p.out = d_out; p.ws = d_ws;
  fused::fused_fwd<<<256, 256, 0, stream>>>(p);
}
```
